```python
import torch

def _softmax(x, axis=-1):
    return torch.softmax(x, dim=axis)
def _gelu(x):
    return torch.nn.functional.gelu(x, approximate="tanh")
def _silu(x):
    return torch.nn.functional.silu(x)
def _sigmoid(x):
    return torch.sigmoid(x)
def _layernorm(x, axis=-1, eps=1e-5):
    m = x.mean(dim=axis, keepdim=True)
    v = x.var(dim=axis, keepdim=True, unbiased=False)
    return (x - m) / torch.sqrt(v + eps)
def _scatter_add(tgt, idx, src, axis=0):
    return tgt.clone().index_add_(axis, idx, src)
def _segment_sum(src, seg, n):
    out = torch.zeros((n,) + src.shape[1:], dtype=src.dtype)
    return out.index_add_(0, seg, src)
def _pad(x, pw):
    flat = tuple(v for pair in reversed(pw) for v in pair)
    return torch.nn.functional.pad(x, flat)

kernel_name = 'concat_heads__ssm_conv_s5__compressed_kv_cla__prompt'

def setup_inputs():

    _w = (lambda a: (lambda t: t.to(torch.bfloat16) if t.is_floating_point() else t)(torch.as_tensor(a)))
    def _A_setup():
        B, S, D = 1, 2048, 640
        P = D
        Lambda_re = -0.5 * (lambda _x: torch.exp(torch.as_tensor(_x)))((lambda *s: torch.randn(*s, dtype=torch.bfloat16) * 0.2)(P))
        Lambda_im = (lambda *s: torch.randn(*s, dtype=torch.bfloat16) * 0.4)(P) * 1.0
        B_re = (lambda *s: torch.randn(*s, dtype=torch.bfloat16) * 0.2)(P, D) * (D ** -0.5)
        B_im = (lambda *s: torch.randn(*s, dtype=torch.bfloat16) * 0.2)(P, D) * (D ** -0.5)
        C_re = (lambda *s: torch.randn(*s, dtype=torch.bfloat16) * 0.2)(D, P) * (P ** -0.5)
        C_im = (lambda *s: torch.randn(*s, dtype=torch.bfloat16) * 0.2)(D, P) * (P ** -0.5)
        log_dt = (lambda *s: torch.randn(*s, dtype=torch.bfloat16) * 0.2)(P) * 0.3 - 2.0
        Dskip = (lambda *s: torch.randn(*s, dtype=torch.bfloat16) * 0.05)(D)
        return {
            "x": _w((lambda *s: torch.randn(*s, dtype=torch.bfloat16) * 0.05)(B, S, D)),
            "Lambda_re": _w(Lambda_re),
            "Lambda_im": _w(Lambda_im),
            "B_re": _w(B_re),
            "B_im": _w(B_im),
            "C_re": _w(C_re),
            "C_im": _w(C_im),
            "log_dt": _w(log_dt),
            "Dskip": _w(Dskip),
        }
    _A_in = _A_setup()

    def _B_setup():
        B, H, Sq, Sk, D, M = 1, 32, 2048, 2048, 128, 640
        Dc = 4 * D
        return {
            "x": _w((lambda *s: torch.randn(*s, dtype=torch.bfloat16) * 0.8)(B, Sq, M)),
            "c_kv": _w((lambda *s: torch.randn(*s, dtype=torch.bfloat16) * 0.2)(B, Sk, Dc)),
            "w_q": _w((lambda *s: torch.randn(*s, dtype=torch.bfloat16) * 3.2)(M, H, D) * (M ** -0.5)),
            "w_uk": _w((lambda *s: torch.randn(*s, dtype=torch.bfloat16) * 3.2)(Dc, H, D) * (Dc ** -0.5)),
            "w_uv": _w((lambda *s: torch.randn(*s, dtype=torch.bfloat16) * 0.2)(Dc, H, D) * (Dc ** -0.5)),
            "w_o": _w((lambda *s: torch.randn(*s, dtype=torch.bfloat16) * 0.2)(H, D, M) * ((H * D) ** -0.5)),
        }
    _B_in = _B_setup()

    return {
        "A_x": _A_in["x"],
        "A_Lambda_re": _A_in["Lambda_re"],
        "A_Lambda_im": _A_in["Lambda_im"],
        "A_B_re": _A_in["B_re"],
        "A_B_im": _A_in["B_im"],
        "A_C_re": _A_in["C_re"],
        "A_C_im": _A_in["C_im"],
        "A_log_dt": _A_in["log_dt"],
        "A_Dskip": _A_in["Dskip"],
        "B_x": _B_in["x"],
        "B_c_kv": _B_in["c_kv"],
        "B_w_q": _B_in["w_q"],
        "B_w_uk": _B_in["w_uk"],
        "B_w_uv": _B_in["w_uv"],
        "B_w_o": _B_in["w_o"],
    }

def reference(A_x, A_Lambda_re, A_Lambda_im, A_B_re, A_B_im, A_C_re, A_C_im, A_log_dt, A_Dskip, B_x, B_c_kv, B_w_q, B_w_uk, B_w_uv, B_w_o):
    def _A_ref(x, Lambda_re, Lambda_im, B_re, B_im, C_re, C_im, log_dt, Dskip):
        Bsz, L, H = x.shape
        Lam = Lambda_re + 1j * Lambda_im
        Bmat = B_re + 1j * B_im
        Cmat = C_re + 1j * C_im
        dt = torch.exp(log_dt)
        Lam_bar = torch.exp(dt * Lam)
        B_bar = ((Lam_bar - 1.0) / Lam)[:, None] * Bmat
        Bu = torch.einsum("bld,pd->blp", x + 0j, B_bar)
        h = torch.zeros_like(Bu[:, 0, :])
        hs = []
        for t in range(L):
            h = Lam_bar[None, :] * h + Bu[:, t, :]
            hs.append(h)
        hseq = torch.stack(hs, 1)
        y = 2.0 * torch.einsum("blp,dp->bld", hseq, Cmat).real + x * Dskip[None, None, :]
        return y

    def _B_ref(x, c_kv, w_q, w_uk, w_uv, w_o):
        B, Sq, M = x.shape[0], x.shape[1], x.shape[2]
        Sk = c_kv.shape[1]
        H, D = w_q.shape[1], w_q.shape[2]
        q = torch.einsum("bqm,mhd->bhqd", x, w_q)
        k = torch.einsum("bkc,chd->bhkd", c_kv, w_uk)
        v = torch.einsum("bkc,chd->bhkd", c_kv, w_uv)
        scores = torch.einsum("bhqd,bhkd->bhqk", q, k) * (D ** -0.5)
        off = Sk - Sq
        qi = torch.arange(Sq)[:, None]
        kj = torch.arange(Sk)[None, :]
        cmask = kj <= (qi + off)
        scores = torch.where(cmask[None, None], scores, -1e9)
        p = _softmax(scores, axis=-1)
        ctx = torch.einsum("bhqk,bhkd->bhqd", p, v)
        return torch.einsum("bhqd,hdm->bqm", ctx, w_o)

    a = _A_ref(A_x, A_Lambda_re, A_Lambda_im, A_B_re, A_B_im, A_C_re, A_C_im, A_log_dt, A_Dskip)
    b = _B_ref(B_x, B_c_kv, B_w_q, B_w_uk, B_w_uv, B_w_o)
    return torch.concatenate([a, b], axis=-1)
```

```cpp
#include <hip/hip_runtime.h>
#include <hip/hip_bf16.h>
#include <stdint.h>

typedef unsigned short u16;
__device__ __forceinline__ float bf2f(u16 b) { return __uint_as_float(((unsigned)b) << 16); }
__device__ __forceinline__ u16 f2bf(float f) { __bf16 h = (__bf16)f; return __builtin_bit_cast(u16, h); }

template <int BT, int OUTF32>
__global__ void gemm_naive(const u16* A, const u16* B, void* C, int M, int N, int K, int ldc) {
    __shared__ float As[32][33], Bs[32][33];
    int tx = threadIdx.x & 31, ty = threadIdx.x >> 5;
    int m0 = blockIdx.y * 32, n0 = blockIdx.x * 32;
    float acc[4] = {0, 0, 0, 0};
    for (int k0 = 0; k0 < K; k0 += 32) {
        for (int i = ty; i < 32; i += 8) {
            As[i][tx] = bf2f(A[(size_t)(m0 + i) * K + k0 + tx]);
            if (BT) Bs[tx][i] = bf2f(B[(size_t)(n0 + i) * K + k0 + tx]);
            else Bs[i][tx] = bf2f(B[(size_t)(k0 + i) * N + n0 + tx]);
        }
        __syncthreads();
        for (int k = 0; k < 32; ++k) {
            float b = Bs[k][tx];
            for (int r = 0; r < 4; ++r) acc[r] += As[ty * 4 + r][k] * b;
        }
        __syncthreads();
    }
    for (int r = 0; r < 4; ++r) {
        size_t o = (size_t)(m0 + ty * 4 + r) * ldc + n0 + tx;
        if (OUTF32 == 1) ((float*)C)[o] = acc[r];
        else if (OUTF32 == 2) ((float*)C)[o] = bf2f(f2bf(acc[r]));
        else ((u16*)C)[o] = f2bf(acc[r]);
    }
}

__global__ void attn_naive(const u16* Q, const u16* Kk, const u16* V, u16* ctx) {
    __shared__ float p[2048];
    __shared__ float qs[128];
    __shared__ float red[128];
    int q = blockIdx.x, h = blockIdx.y, t = threadIdx.x;
    qs[t] = bf2f(Q[(size_t)q * 4096 + h * 128 + t]);
    __syncthreads();
    float mx = -1e30f;
    for (int j = t; j <= q; j += 128) {
        float s = 0;
        const u16* kr = Kk + (size_t)j * 4096 + h * 128;
        for (int d = 0; d < 128; ++d) s += qs[d] * bf2f(kr[d]);
        s *= 0.08838834764831845f;
        p[j] = s; mx = fmaxf(mx, s);
    }
    red[t] = mx; __syncthreads();
    for (int o = 64; o > 0; o >>= 1) { if (t < o) red[t] = fmaxf(red[t], red[t + o]); __syncthreads(); }
    mx = red[0]; __syncthreads();
    float sum = 0;
    for (int j = t; j <= q; j += 128) { float e = __expf(p[j] - mx); p[j] = e; sum += e; }
    red[t] = sum; __syncthreads();
    for (int o = 64; o > 0; o >>= 1) { if (t < o) red[t] += red[t + o]; __syncthreads(); }
    sum = red[0];
    float acc = 0;
    for (int j = 0; j <= q; ++j) acc += p[j] * bf2f(V[(size_t)j * 4096 + h * 128 + t]);
    ctx[(size_t)q * 4096 + h * 128 + t] = f2bf(acc / sum);
}

__global__ void scan_naive(const float* Ure, const float* Uim, const u16* Lre, const u16* Lim, const u16* logdt, float* hre, float* him) {
    int p = blockIdx.x * blockDim.x + threadIdx.x;
    if (p >= 640) return;
    float lr = bf2f(Lre[p]), li = bf2f(Lim[p]);
    float dt = bf2f(f2bf(expf(bf2f(logdt[p]))));
    float a = dt * lr, b = dt * li;
    float e = expf(a), br = e * cosf(b), bi = e * sinf(b);
    float nr = br - 1.f, ni = bi, den = lr * lr + li * li;
    float cr = (nr * lr + ni * li) / den, ci = (ni * lr - nr * li) / den;
    float hr = 0, hi = 0;
    for (int t = 0; t < 2048; ++t) {
        float ur = Ure[t * 640 + p], ui = Uim[t * 640 + p];
        float xr = cr * ur - ci * ui, xi = cr * ui + ci * ur;
        float nhr = br * hr - bi * hi + xr, nhi = br * hi + bi * hr + xi;
        hr = nhr; hi = nhi;
        hre[t * 640 + p] = hr; him[t * 640 + p] = hi;
    }
}
__global__ void cproj_naive(const float* hre, const float* him, const u16* Cre, const u16* Cim, const u16* x, const u16* Dskip, float* out) {
    __shared__ float hr[640], hi[640];
    int t = blockIdx.x;
    for (int i = threadIdx.x; i < 640; i += blockDim.x) { hr[i] = hre[t * 640 + i]; hi[i] = him[t * 640 + i]; }
    __syncthreads();
    for (int d = threadIdx.x; d < 640; d += blockDim.x) {
        float acc = 0;
        for (int p = 0; p < 640; ++p) acc += hr[p] * bf2f(Cre[d * 640 + p]) - hi[p] * bf2f(Cim[d * 640 + p]);
        out[(size_t)t * 1280 + d] = 2.f * acc + bf2f(f2bf(bf2f(x[t * 640 + d]) * bf2f(Dskip[d])));
    }
}

extern "C" void kernel_launch(void* const* d_in, const int* in_sizes, int n_in, void* d_out, int out_size, void* d_ws, size_t ws_size, hipStream_t stream) {
    const u16* A_x = (const u16*)d_in[0]; const u16* Lre = (const u16*)d_in[1]; const u16* Lim = (const u16*)d_in[2];
    const u16* B_re = (const u16*)d_in[3]; const u16* B_im = (const u16*)d_in[4]; const u16* C_re = (const u16*)d_in[5]; const u16* C_im = (const u16*)d_in[6];
    const u16* logdt = (const u16*)d_in[7]; const u16* Dskip = (const u16*)d_in[8];
    const u16* B_x = (const u16*)d_in[9]; const u16* ckv = (const u16*)d_in[10]; const u16* wq = (const u16*)d_in[11]; const u16* wuk = (const u16*)d_in[12];
    const u16* wuv = (const u16*)d_in[13]; const u16* wo = (const u16*)d_in[14];
    float* out = (float*)d_out;
    char* ws = (char*)d_ws;
    u16* Q = (u16*)ws; u16* K = (u16*)(ws + (16u << 20)); u16* V = (u16*)(ws + (32u << 20)); u16* ctx = (u16*)(ws + (48u << 20));
    float* Ure = (float*)(ws + (64u << 20)); float* Uim = (float*)(ws + (72u << 20)); float* hre = (float*)(ws + (80u << 20)); float* him = (float*)(ws + (88u << 20));
    gemm_naive<0, 0><<<dim3(4096 / 32, 2048 / 32), 256, 0, stream>>>(B_x, wq, Q, 2048, 4096, 640, 4096);
    gemm_naive<0, 0><<<dim3(4096 / 32, 2048 / 32), 256, 0, stream>>>(ckv, wuk, K, 2048, 4096, 512, 4096);
    gemm_naive<0, 0><<<dim3(4096 / 32, 2048 / 32), 256, 0, stream>>>(ckv, wuv, V, 2048, 4096, 512, 4096);
    gemm_naive<1, 1><<<dim3(640 / 32, 2048 / 32), 256, 0, stream>>>(A_x, B_re, Ure, 2048, 640, 640, 640);
    gemm_naive<1, 1><<<dim3(640 / 32, 2048 / 32), 256, 0, stream>>>(A_x, B_im, Uim, 2048, 640, 640, 640);
    attn_naive<<<dim3(2048, 32), 128, 0, stream>>>(Q, K, V, ctx);
    scan_naive<<<10, 64, 0, stream>>>(Ure, Uim, Lre, Lim, logdt, hre, him);
    cproj_naive<<<2048, 128, 0, stream>>>(hre, him, C_re, C_im, A_x, Dskip, out);
    gemm_naive<0, 2><<<dim3(640 / 32, 2048 / 32), 256, 0, stream>>>(ctx, wo, out + 640, 2048, 640, 4096, 1280);
}
```

```cpp
#include <hip/hip_runtime.h>
#include <stdint.h>
#include <utility>

typedef unsigned short u16;
typedef float f32x16 __attribute__((ext_vector_type(16)));
typedef float f32x4 __attribute__((ext_vector_type(4)));
typedef float f32x2 __attribute__((ext_vector_type(2)));
typedef short s16x8 __attribute__((ext_vector_type(8)));
typedef short s16x4 __attribute__((ext_vector_type(4)));
typedef __bf16 bf16x2 __attribute__((ext_vector_type(2)));
typedef unsigned u32x4 __attribute__((ext_vector_type(4)));
typedef unsigned u32x2 __attribute__((ext_vector_type(2)));
template <int... Is> using iseq = std::integer_sequence<int, Is...>;
template <int N> using make_iseq = std::make_integer_sequence<int, N>;
#define DI __device__ __forceinline__
#define LAS __attribute__((address_space(3)))
#define GAP() __builtin_amdgcn_sched_barrier(0)
typedef LAS const char* ldsp;

DI unsigned rfl(unsigned x) { return __builtin_amdgcn_readfirstlane(x); }
DI unsigned cvtpk(float lo, float hi) { f32x2 v = {lo, hi}; bf16x2 b = __builtin_convertvector(v, bf16x2); return __builtin_bit_cast(unsigned, b); }
DI float bf2f(u16 b) { return __uint_as_float(((unsigned)b) << 16); }
DI u16 f2bf(float f) { __bf16 h = (__bf16)f; return __builtin_bit_cast(u16, h); }
DI s16x8 lds_b128(ldsp p) { return *(const LAS s16x8*)p; }
DI s16x4 lds_tr(ldsp p) { return __builtin_amdgcn_ds_read_tr16_b64_v4i16((LAS s16x4*)p); }
DI s16x8 cat8(s16x4 a, s16x4 b) { return __builtin_shufflevector(a, b, 0, 1, 2, 3, 4, 5, 6, 7); }
#define SRD3 0x00020000
DI __amdgpu_buffer_rsrc_t mk_srd(const void* p, unsigned bytes) {
    uint64_t a = (uint64_t)p; unsigned lo = rfl((unsigned)a), hi = rfl((unsigned)(a >> 32));
    return __builtin_amdgcn_make_buffer_rsrc((void*)(((uint64_t)hi << 32) | lo), (short)0, (int)bytes, SRD3);
}
DI void dma16(unsigned m0v, unsigned voff, __amdgpu_buffer_rsrc_t srd, unsigned soff) {
    asm volatile("s_mov_b32 m0, %0\n\ts_nop 0\n\tbuffer_load_dwordx4 %1, %2, %3 offen lds" ::"s"(m0v), "v"(voff), "s"(srd), "s"(soff) : "memory");
}
DI void mfma16v(f32x4& acc, const s16x8& a, const s16x8& b) { asm volatile("v_mfma_f32_16x16x32_bf16 %0, %1, %2, %0" : "+v"(acc) : "v"(a), "v"(b)); }
DI void st128(u32x4 w, __amdgpu_buffer_rsrc_t srd, unsigned voff, unsigned soff) { __builtin_amdgcn_raw_buffer_store_b128(w, srd, voff, soff, 16); asm volatile("s_nop 1" ::"v"(w) : "memory"); }
DI unsigned otid() { unsigned t = threadIdx.x; asm volatile("" : "+v"(t)); return t; }
template <int A, int OFF> DI void ld_acc(unsigned voff, __amdgpu_buffer_rsrc_t srd, unsigned soff) {
    asm volatile("buffer_load_dwordx4 a[%c3:%c4], %0, %1, %2 offen offset:%c5 sc1" ::"v"(voff), "s"(srd), "s"(soff), "i"(A), "i"(A + 3), "i"(OFF) : "memory");
}
template <int A, int OFF> DI void ld_acc0(unsigned voff, __amdgpu_buffer_rsrc_t srd, unsigned soff) {
    asm volatile("buffer_load_dwordx4 a[%c3:%c4], %0, %1, %2 offen offset:%c5" ::"v"(voff), "s"(srd), "s"(soff), "i"(A), "i"(A + 3), "i"(OFF) : "memory");
}
DI void dma16nt(unsigned m0v, unsigned voff, __amdgpu_buffer_rsrc_t srd, unsigned soff) {
    asm volatile("s_mov_b32 m0, %0\n\ts_nop 0\n\tbuffer_load_dwordx4 %1, %2, %3 offen nt lds" ::"s"(m0v), "v"(voff), "s"(srd), "s"(soff) : "memory");
}

#define ACLOB_LO "a0","a1","a2","a3","a4","a5","a6","a7","a8","a9","a10","a11","a12","a13","a14","a15","a16","a17","a18","a19","a20","a21","a22","a23","a24","a25","a26","a27","a28","a29","a30","a31","a32","a33","a34","a35","a36","a37","a38","a39","a40","a41","a42","a43","a44","a45","a46","a47","a48","a49","a50","a51","a52","a53","a54","a55","a56","a57","a58","a59","a60","a61","a62","a63","a64","a65","a66","a67","a68","a69","a70","a71","a72","a73","a74","a75","a76","a77","a78","a79","a80","a81","a82","a83","a84","a85","a86","a87","a88","a89","a90","a91","a92","a93","a94","a95","a96","a97","a98","a99","a100","a101","a102","a103","a104","a105","a106","a107","a108","a109","a110","a111","a112","a113","a114","a115","a116","a117","a118","a119","a120","a121","a122","a123","a124","a125","a126","a127"
#define ACLOB_HI "a128","a129","a130","a131","a132","a133","a134","a135","a136","a137","a138","a139","a140","a141","a142","a143","a144","a145","a146","a147","a148","a149","a150","a151","a152","a153","a154","a155","a156","a157","a158","a159","a160","a161","a162","a163","a164","a165","a166","a167","a168","a169","a170","a171","a172","a173","a174","a175","a176","a177","a178","a179","a180","a181","a182","a183","a184","a185","a186","a187","a188","a189","a190","a191","a192","a193","a194","a195","a196","a197","a198","a199","a200","a201","a202","a203","a204","a205","a206","a207","a208","a209","a210","a211","a212","a213","a214","a215","a216","a217","a218","a219","a220","a221","a222","a223","a224","a225","a226","a227","a228","a229","a230","a231","a232","a233","a234","a235","a236","a237","a238","a239","a240","a241","a242","a243","a244","a245","a246","a247","a248","a249","a250","a251","a252","a253","a254","a255"
#define ACLOB_ALL ACLOB_LO, ACLOB_HI
#define ACC_GUARD() asm volatile("" ::: ACLOB_ALL)

constexpr size_t WS_CNT = 0, WS_K = 1u << 20, WS_V = (20u << 20) + 8192, WS_CTX = 40u << 20, WS_HT = 60u << 20, WS_SLAB = 68u << 20, WS_WOT = 88u << 20, WS_SEND = 100u << 20;
constexpr int C_KV = 1, C_U = 33, C_WO = 47, C_H = 48, C_CTX = 64, C_RED = 160;
constexpr float QSCALE = 0.08838834764831845f * 1.4426950408889634f;
constexpr unsigned HSTR = 524288 + 16384, CT_S = 1048576 + 16384, HT_S = 327680 + 16384;
DI unsigned* cnt_ptr(char* ws, int i) { return (unsigned*)(ws + WS_CNT) + i * 64; }
DI void sig_tag(unsigned* p, unsigned ep) { __hip_atomic_store(p, ep, __ATOMIC_RELAXED, __HIP_MEMORY_SCOPE_AGENT); }
DI bool tags_ok(unsigned v, unsigned n, unsigned ep) { return __all((otid() & 63u) >= n || v == ep); }
DI void poll_tags(unsigned* p, unsigned n, unsigned ep) {
    for (unsigned spin = 0; spin < 300000u; ++spin) { if (tags_ok(__hip_atomic_load(p + (otid() & 63u), __ATOMIC_RELAXED, __HIP_MEMORY_SCOPE_AGENT), n, ep)) break; __builtin_amdgcn_s_sleep(2); }
}
DI unsigned flag_issue(unsigned* p) { unsigned v; asm volatile("global_load_dword %0, %1, off sc1" : "=v"(v) : "v"(p + (otid() & 63u)) : "memory"); return v; }
DI void flag_check(unsigned v, unsigned* p, unsigned n, unsigned ep) {
    asm volatile("" : "+v"(v));
    if (!tags_ok(v, n, ep)) poll_tags(p, n, ep);
    asm volatile("" ::: "memory");
}
struct KArgs { const u16 *ax, *lre, *lim, *bre, *bim, *cre, *cim, *logdt, *dskip, *bx, *ckv, *wq, *wuk, *wuv, *wo; float* out; char* ws; };
typedef const __attribute__((address_space(4))) KArgs* kargp;
DI kargp kargs() { kargp p = (kargp)__builtin_amdgcn_kernarg_segment_ptr(); asm volatile("" : "+s"(p)); return p; }

constexpr unsigned WROW = 8192;
template <int R> DI float a_read() { float r; asm volatile("v_accvgpr_read_b32 %0, a[%c1]" : "=v"(r) : "i"(R)); return r; }
template <int T> DI f32x4 a_read4() { f32x4 r; r[0] = a_read<T>(); r[1] = a_read<T + 1>(); r[2] = a_read<T + 2>(); r[3] = a_read<T + 3>(); return r; }
template <int I> DI void a_zero16v(const u32x4& z) { asm volatile("v_mfma_f32_32x32x16_bf16 a[%c1:%c2], %0, %0, 0" ::"v"(z), "i"(16 * I), "i"(16 * I + 15)); }
template <int... Is> DI void a_zero_n(iseq<Is...>) { u32x4 z = {0u, 0u, 0u, 0u}; asm volatile("s_nop 1" : "+v"(z)); (a_zero16v<Is>(z), ...); }
struct KvC {
    ldsp va[2], vw[2];
    unsigned vact[2], vwsrc[2];
    unsigned m0a, m0w, soa, sow, m0a2, m0w1;
    __amdgpu_buffer_rsrc_t srdA, srdW[2];
    s16x8 X[2][8], Y[2][8];
    uint64_t pft;
    unsigned pfl[3], pfh[3], pfm0, pfv, cw, gw, hx;
};
DI void kv_pf0(KvC& c) {
    uint64_t pc; unsigned len, back;
    asm volatile("s_getpc_b64 %0\n1:\n\ts_mov_b32 %1, .Lmega_end-.Lmega_pc\n\ts_mov_b32 %2, 1b-.Lmega_pc" : "=s"(pc), "=s"(len), "=s"(back));
    dma16(c.pfm0, c.pfv, mk_srd((const void*)((pc - back) & ~(uint64_t)15), len), c.cw * 1024u);
    dma16(c.pfm0, c.pfv, c.srdA, c.gw * 2048u); dma16(c.pfm0, c.pfv, c.srdA, c.gw * 2048u + 1024u);
#pragma unroll
    for (unsigned i = 1; i < 4; ++i) { const unsigned so = (128u * i + c.cw) * WROW + c.hx; dma16nt(c.pfm0, c.pfv, c.srdW[0], so); dma16nt(c.pfm0, c.pfv, c.srdW[1], so); }
}
template <int Q> DI void kv_pf_addr(KvC& c, unsigned kt) {
    const unsigned i = Q < 2 ? (2u * kt + Q + 12u) & 15u : 16u + kt;
    c.pfl[Q] = (unsigned)__builtin_amdgcn_readlane((int)(unsigned)c.pft, (int)i); c.pfh[Q] = (unsigned)__builtin_amdgcn_readlane((int)(unsigned)(c.pft >> 32), (int)i);
}
template <int Q> DI void kv_pf(KvC& c) {
    const uint64_t a = ((uint64_t)c.pfh[Q] << 32) | c.pfl[Q];
    if constexpr (Q < 2) asm volatile("s_mov_b32 m0, %0\n\ts_nop 0\n\tglobal_load_lds_dwordx4 %1, %2" ::"s"(c.pfm0), "v"(c.pfv), "s"(a) : "memory");
    else asm volatile("s_mov_b32 m0, %0\n\ts_nop 0\n\tglobal_load_lds_dwordx4 %1, %2 nt" ::"s"(c.pfm0), "v"(c.pfv), "s"(a) : "memory");
}
template <bool TRANS, int X_, int Y_, int BUF> DI void kv_mfma(KvC& c) {
    constexpr int ao = 4 * (8 * X_ + Y_);
    if constexpr (TRANS) asm volatile("v_mfma_f32_16x16x32_bf16 a[%c2:%c3], %0, %1, a[%c2:%c3]" ::"v"(c.Y[BUF][X_]), "v"(c.X[BUF][Y_]), "i"(ao), "i"(ao + 3));
    else asm volatile("v_mfma_f32_16x16x32_bf16 a[%c2:%c3], %0, %1, a[%c2:%c3]" ::"v"(c.X[BUF][X_]), "v"(c.Y[BUF][Y_]), "i"(ao), "i"(ao + 3));
}
template <bool TRANS, int ST, int K32, int BUF, int J> DI void kv_read(KvC& c, s16x4& lo) {
    constexpr int JJ = TRANS ? (J < 8 ? J + 16 : J - 8) : J;
    if constexpr (JJ >= 16) c.X[BUF][JJ - 16] = lds_b128(c.va[K32] + ST * 32768 + (JJ - 16) * 2048);
    else { constexpr int cb = JJ >> 1, o = ST * 32768 + K32 * 16384 + (cb >> 1) * 512;
        if constexpr ((JJ & 1) == 0) lo = lds_tr(c.vw[cb & 1] + o); else c.Y[BUF][cb] = cat8(lo, lds_tr(c.vw[cb & 1] + o + 256)); }
}
template <int ST, int P, bool INL = false> DI void kv_dma(KvC& c, unsigned kt) {
    const unsigned oob = kt < 8u ? 0u : 0x40000000u;
    if constexpr (P < 8) dma16((INL ? c.m0a2 : c.m0a) + ST * 32768 + P * 1024, c.vact[P & 1], c.srdA, c.soa + kt * 128u + P * 8192u + oob);
    else { constexpr int q = P - 8; dma16((INL ? c.m0w1 : c.m0w) + ST * 32768 + q * 1024, c.vwsrc[q >> 2], c.srdW[(q >> 1) & 1], c.sow + kt * (64u * WROW) + (q >> 2) * (8u * WROW) + (q & 1) * 128u + oob); }
}
struct KvOut { __amdgpu_buffer_rsrc_t srd; unsigned vout, sA, sB; };
template <int F> DI void kv_store(const KvOut& o) {
    constexpr int xp = F >> 3, y = F & 7;
    const f32x4 a = a_read4<4 * (16 * xp + y)>(), b = a_read4<4 * (16 * xp + 8 + y)>();
    u32x4 w; w[0] = cvtpk(a[0], a[1]); w[1] = cvtpk(a[2], a[3]); w[2] = cvtpk(b[0], b[1]); w[3] = cvtpk(b[2], b[3]);
    st128(w, o.srd, o.vout, xp * o.sA + y * o.sB);
}
template <int F0, int... Fs> DI void kv_store_n(const KvOut& o, iseq<Fs...>) { (kv_store<F0 + Fs>(o), ...); }
template <bool TRANS, int ST, int G> DI void kv_gap(KvC& c, unsigned kt, s16x4& lo) {
    constexpr int s = G >> 6, i = G & 63;
    kv_mfma<TRANS, (i >> 3), (i & 7), s>(c);
    GAP();
    if constexpr (G == 65) ACC_GUARD();
    if constexpr (G == 0) { c.m0w1 = kt < 7u ? c.m0w : 0x30000u; c.m0a2 = kt < 6u ? c.m0a : 0x30000u; }
    if constexpr (ST == 0 && G == 31) { if (kt == 0) kv_pf0(c); }
    if constexpr ((G == 32 || G == 34 || G == 36)) { kv_pf_addr<(G - 32) / 2>(c, kt); asm volatile("" : "+s"(c.pfl[(G - 32) / 2]), "+s"(c.pfh[(G - 32) / 2])); }
    if constexpr ((G == 48 || G == 52 || G == 56)) kv_pf<(G - 48) / 4>(c);
    if constexpr (G == 64) { if constexpr (ST == 0) asm volatile("s_cmp_eq_u32 %0, 0\n\ts_cbranch_scc1 1f\n\ts_waitcnt vmcnt(3)\n1:\n\ts_waitcnt vmcnt(12)" ::"s"(kt) : "memory", "scc"); else asm volatile("s_cmp_eq_u32 %0, 7\n\ts_cbranch_scc1 1f\n\ts_waitcnt vmcnt(%c1)\n1:" ::"s"(kt), "i"(3) : "memory", "scc"); __builtin_amdgcn_s_waitcnt(0xC07F); __builtin_amdgcn_s_barrier(); asm volatile("" ::: "memory"); }
    if constexpr ((i & 1) && i < 48) { if constexpr (s == 0) kv_read<TRANS, ST, 1, 1, (i >> 1)>(c, lo); else kv_read<TRANS, ST ^ 1, 0, 0, (i >> 1)>(c, lo); }
    if constexpr (s == 0 && i < 32 && (i & 3) == 2) kv_dma<ST ^ 1, 8 + (i >> 2), true>(c, kt + 1);
    if constexpr (s == 1 && (i & 7) == 2) kv_dma<ST, (i >> 3), true>(c, kt + 2);
    GAP();
}
template <bool TRANS, int ST, int... Gs> DI void kv_step_(KvC& c, unsigned kt, iseq<Gs...>) { s16x4 lo; (kv_gap<TRANS, ST, Gs>(c, kt, lo), ...); }
template <bool TRANS, int ST> DI void kv_step(KvC& c, unsigned kt) { kv_step_<TRANS, ST>(c, kt, make_iseq<128>{}); }
template <int ST, int... Ps> DI void kv_dma_all(KvC& c, unsigned kt, iseq<Ps...>) { (kv_dma<ST, Ps>(c, kt), ...); }
template <bool TRANS, int... Js> DI void kv_read0(KvC& c, iseq<Js...>) { s16x4 lo; (kv_read<TRANS, 0, 0, 0, Js>(c, lo), ...); }
DI unsigned kv_ready(unsigned v, unsigned ep) { return (unsigned)__builtin_ctzll(~__ballot(v == ep) | (1ull << 32)) >> 2; }
DI void kv_wait(unsigned& rdy, unsigned need, unsigned* kvf, unsigned ep) {
    for (unsigned spin = 0; rdy <= need && spin < 300000u; ++spin) { rdy = kv_ready(__hip_atomic_load(kvf + (otid() & 63u), __ATOMIC_RELAXED, __HIP_MEMORY_SCOPE_AGENT), ep); if (rdy <= need) __builtin_amdgcn_s_sleep(2); }
    asm volatile("" ::: "memory");
}
template <bool TRANS, class HOOK> DI void kv_main(KvC& c, HOOK&& hook) {
    kv_read0<TRANS>(c, make_iseq<24>{});
    GAP();
        for (unsigned kt = 0; kt < 8; kt += 2) { kv_step<TRANS, 0>(c, kt); if (kt == 6) hook(); kv_step<TRANS, 1>(c, kt + 1); }
}
template <class HOOK> DI void kv_tile(HOOK&& hook, KvOut& o, char* lds, const u16* ckv, const u16* wuk, const u16* wuv, char* ws, int h, int j) {
    const unsigned tid = otid(), lane = tid & 63u, wid = rfl(tid >> 6), r16 = lane & 15u, g = lane >> 4, hi = lane >> 5, tr = wid & 1u, half = wid >> 1;
    const unsigned ldsb = (unsigned)(size_t)lds;
    KvC c;
    #pragma unroll
    for (int par = 0; par < 2; ++par) { c.vact[par] = (lane >> 3) * 1024u + (((lane & 7u) ^ ((4u * par + (lane >> 4)) & 7u)) * 16u);
        c.vwsrc[par] = ((lane >> 2) & 7u) * WROW + hi * 64u + ((lane & 3u) ^ (2u * par)) * 16u; }
    c.m0a = rfl(ldsb + wid * 8192u); c.m0w = rfl(ldsb + 65536u + wid * 8192u);
    c.soa = rfl((unsigned)(256 * j + 64 * wid) * 1024u); c.sow = rfl(16u * wid * WROW + (unsigned)h * 256u);
    c.srdA = mk_srd(ckv, 2048u * 1024u); c.srdW[0] = mk_srd(wuk, 512u * WROW); c.srdW[1] = mk_srd(wuv, 512u * WROW);
    asm volatile("s_nop 4" ::: "memory", ACLOB_ALL);
    kv_dma_all<0>(c, 0u, make_iseq<16>{});
    {
        kv_dma_all<1>(c, 1u, make_iseq<8>{});
        asm volatile(".Lmega_pc:");
        const unsigned cw = rfl((unsigned)(((h & 3) * 8 + j) * 4) + wid);
        const unsigned gw = rfl(blockIdx.x * 4u + wid); c.pfm0 = rfl(ldsb + 131072u + wid * 1024u); c.pfv = lane * 16u;
        c.cw = cw; c.gw = gw; c.hx = (unsigned)(h >> 2) * 1024u;
        const kargp a = kargs();
        uint64_t p0 = (uint64_t)a->ax, p1 = (uint64_t)a->bre, p2 = (uint64_t)a->bim, p3 = (uint64_t)a->bx, p4 = (uint64_t)a->cre, p5 = (uint64_t)a->cim, p6 = (uint64_t)a->wq;
        asm volatile("" : "+s"(p0), "+s"(p1), "+s"(p2), "+s"(p3), "+s"(p4), "+s"(p5), "+s"(p6));
        const uint64_t base = lane < 3u ? p0 : lane == 3u ? p1 : lane == 4u ? p2 : lane < 8u ? p3 : lane == 8u ? p4 : lane == 9u ? p5 : lane < 19u ? (uint64_t)ckv : p6;
        const unsigned np = (lane < 3u || (lane >= 5u && lane < 8u)) ? 2560u : 800u, pi = (lane < 3u ? lane : (lane >= 5u && lane < 8u) ? lane - 5u : 0u) * 1024u + gw;
        const unsigned off = lane >= 19u ? ((lane < 24u ? lane - 19u : 0u) * 128u + cw) * WROW + (unsigned)(h >> 2) * 1024u : (pi < np ? pi : gw & 511u) * 1024u;
        c.pft = base + off;
    }
#pragma unroll
    for (int kk = 0; kk < 2; ++kk) c.va[kk] = (ldsp)(size_t)(ldsb + (128u * tr + r16) * 128u + (((4u * kk + g) ^ ((r16 >> 1) & 7u)) * 16u));
#pragma unroll
    for (int p = 0; p < 2; ++p) c.vw[p] = (ldsp)(size_t)(ldsb + 65536u + g * 4096u + half * 2048u + ((p ^ (g & 1u)) * 32u) + ((lane >> 2) & 3u) * 64u + (lane & 3u) * 8u);
    a_zero_n(make_iseq<16>{});
    asm volatile("s_waitcnt vmcnt(%c0)\n\ts_barrier" ::"i"(8) : "memory");
    if (half == 0) kv_main<true>(c, hook); else kv_main<false>(c, hook);
    asm volatile("s_nop 7" ::: "memory");
    o.srd = mk_srd(ws + (half ? WS_V : WS_K) + (size_t)h * HSTR + j * 65536 + tr * 32768u, 32768u); o.vout = lane * 16u; o.sA = half ? 8192u : 1024u; o.sB = half ? 1024u : 4096u;
        }

template <int N> DI void dma4(unsigned m0v, unsigned voff, __amdgpu_buffer_rsrc_t srd, unsigned soff) {
    if constexpr (N == 0) asm volatile("s_mov_b32 m0, %0\n\ts_nop 0\n\tbuffer_load_dwordx4 %1, %2, %3 offen sc1 lds" ::"s"(m0v), "v"(voff), "s"(srd), "s"(soff) : "memory");
    else asm volatile("buffer_load_dwordx4 %0, %1, %2 offen offset:%c3 sc1 lds" ::"v"(voff), "s"(srd), "s"(soff), "i"(N * 1024) : "memory");
}
constexpr unsigned Q_STG = 49152;
constexpr unsigned L_XA = 0, L_V2 = 32768, L_K0 = 49152, L_K1 = 65536, L_V0 = 81920, L_K2 = 98304, L_V1 = 114688, L_XB = 131072;
struct QpC {
    ldsp va[2], vw[2];
    unsigned vact[2], vwsrc[2], voff;
    unsigned m0a, m0w, soa, sow, m0r, sor;
    unsigned qdst;
    KvOut ko; bool kst;
    __amdgpu_buffer_rsrc_t srdA, srdW, srdK, srdV;
    s16x8 X[2][4], Y[2][8];
};
template <int X_, int Y_, int BUF> DI void qp_mfma(QpC& c) {
    constexpr int ao = 4 * (4 * X_ + Y_);
    asm volatile("v_mfma_f32_16x16x32_bf16 a[%c2:%c3], %0, %1, a[%c2:%c3]" ::"v"(c.Y[BUF][X_]), "v"(c.X[BUF][Y_]), "i"(ao), "i"(ao + 3));
}
template <int K32, int BUF, int J> DI void qp_read(QpC& c, unsigned st, s16x4& lo) {
    if constexpr (J < 4) c.X[BUF][J] = lds_b128(c.va[K32] + st + J * 2048);
    else { constexpr int cb = (J - 4) >> 1, o = K32 * 8192 + (cb >> 1) * 512;
        if constexpr ((J & 1) == 0) lo = lds_tr(c.vw[cb & 1] + st + o); else c.Y[BUF][cb] = cat8(lo, lds_tr(c.vw[cb & 1] + st + o + 256)); }
}
template <int MODE, int P> DI void qp_dma(QpC& c, unsigned st, unsigned kt) {
    if constexpr (MODE == 0) {
        if constexpr (P < 8) dma16(c.m0a + st + P * 1024, c.vact[P & 1], c.srdA, c.soa + kt * 128u + P * (8u * 1280u));
        else { constexpr int q = P - 8; dma16(c.m0w + st + q * 1024, c.vwsrc[q >> 1], c.srdW, c.sow + kt * (64u * WROW) + (q >> 1) * (8u * WROW) + (q & 1) * 128u); }
    } else if constexpr (MODE == 1) {
        if constexpr (P < 4) dma4<P & 3>(c.m0r + L_K0, c.voff, c.srdK, c.sor); else if constexpr (P < 8) dma4<P & 3>(c.m0r + L_K1, c.voff, c.srdK, c.sor + 16384u); else dma4<P & 3>(c.m0r + L_V0, c.voff, c.srdV, c.sor);
    } else if constexpr (MODE == 2) {
        if constexpr (P < 4) dma4<P & 3>(c.m0r + L_K2, c.voff, c.srdK, c.sor + 32768u); else if constexpr (P < 8) dma4<P & 3>(c.m0r + L_V1, c.voff, c.srdV, c.sor + 16384u);
    }
}
template <int F> DI void qp_store(QpC& c) {
    constexpr int ds = F >> 2, y = F & 3;
    const f32x4 a = a_read4<4 * (8 * ds + y)>() * QSCALE, b = a_read4<4 * (8 * ds + 4 + y)>() * QSCALE;
    u32x4 w; w[0] = cvtpk(a[0], a[1]); w[1] = cvtpk(a[2], a[3]); w[2] = cvtpk(b[0], b[1]); w[3] = cvtpk(b[2], b[3]);
    *(LAS u32x4*)(size_t)(c.qdst + y * 4096 + ds * 1024) = w;
}
template <int MODE, int PM, int KS, int G> DI void qp_gap(QpC& c, unsigned st, unsigned stn, unsigned stp, unsigned kt, s16x4& lo) {
    constexpr int s = G >> 5, i = G & 31;
    if constexpr (MODE == 3 && s == 1) qp_mfma<2 * (i >> 3) + (i & 1), (i >> 1) & 3, 1>(c);
    else qp_mfma<(i >> 2), (i & 3), s>(c);
    GAP();
    if constexpr (G == 32) { if constexpr (MODE != 3) asm volatile("s_waitcnt vmcnt(12)" ::: "memory"); __builtin_amdgcn_s_waitcnt(0xC07F); __builtin_amdgcn_s_barrier(); asm volatile("" ::: "memory"); }
    if constexpr (i >= 1 && i <= 20) { if constexpr (s == 0) qp_read<1, 1, i - 1>(c, st, lo); else if constexpr (MODE != 3) qp_read<0, 0, i - 1>(c, stn, lo); }
    if constexpr (s == 1 && i % 5 == 4) qp_dma<MODE, i / 5>(c, st, kt + 3);
    if constexpr (s == 0 && i % 5 == 2) qp_dma<PM, 6 + i / 5>(c, stp, kt + 2);
    if constexpr (G == 33) ACC_GUARD();
    if constexpr (KS >= 0 && G % 10 == 5 && KS + G / 10 < 32) { if (c.kst) kv_store<KS + G / 10>(c.ko); }
    if constexpr (MODE == 3 && s == 1 && i >= 8 && !(i & 1)) qp_store<(i - 8) / 2>(c);
    GAP();
}
template <int MODE, int PM, int KS, int... Gs> DI void qp_step_(QpC& c, unsigned st, unsigned stn, unsigned stp, unsigned kt, iseq<Gs...>) { s16x4 lo; (qp_gap<MODE, PM, KS, Gs>(c, st, stn, stp, kt, lo), ...); }
template <int MODE, int PM, int KS = -1> DI void qp_step(QpC& c, unsigned st, unsigned stn, unsigned stp, unsigned kt) { qp_step_<MODE, PM, KS>(c, st, stn, stp, kt, make_iseq<64>{}); }
template <int... Ps> DI void qp_dma_all(QpC& c, unsigned st, unsigned kt, iseq<Ps...>) { (qp_dma<0, Ps>(c, st, kt), ...); }
template <int... Js> DI void qp_read0(QpC& c, iseq<Js...>) { s16x4 lo; (qp_read<0, 0, Js>(c, 0u, lo), ...); }
DI void q_pro(QpC& c, char* lds, const u16* bx, const u16* wq, int h, int qA, int qB) {
    const unsigned tid = otid(), lane = tid & 63u, wid = rfl(tid >> 6), hi = lane >> 5;
    const unsigned ldsb = (unsigned)(size_t)lds;
#pragma unroll
    for (int par = 0; par < 2; ++par) { c.vact[par] = (lane >> 3) * 1280u + (((lane & 7u) ^ ((4u * par + (lane >> 4)) & 7u)) * 16u);
        c.vwsrc[par] = ((lane >> 2) & 7u) * WROW + hi * 64u + ((lane & 3u) ^ (2u * par)) * 16u; }
    c.m0a = rfl(ldsb + wid * 8192u); c.m0w = rfl(ldsb + 32768u + wid * 4096u);
    c.soa = rfl((unsigned)((wid < 2 ? qA : qB) * 128 + 64 * (int)(wid & 1u)) * 1280u); c.sow = rfl(16u * wid * WROW + (unsigned)h * 256u);
    c.srdA = mk_srd(bx, 2048u * 1280u); c.srdW = mk_srd(wq, 640u * WROW);
    asm volatile("s_nop 4" ::: "memory");
    qp_dma_all(c, 0u, 0u, make_iseq<12>{});
}
DI void q_pro2(QpC& c) { qp_dma_all(c, Q_STG, 1u, make_iseq<12>{}); qp_dma_all(c, 2 * Q_STG, 2u, make_iseq<6>{}); }
template <class UK> DI void u_sload_all(UK& k, char* ws, int ut);
template <class UK> DI unsigned q_main(QpC& c, char* lds, char* ws, int h, UK& uk, int ut, unsigned* sig1, unsigned* sig2, unsigned ep) {
    const unsigned tid = otid(), lane = tid & 63u, wid = rfl(tid >> 6), r16 = lane & 15u, g = lane >> 4;
    const unsigned ldsb = (unsigned)(size_t)lds;
    #pragma unroll
    for (int k = 0; k < 2; ++k) c.va[k] = (ldsp)(size_t)(ldsb + (64u * wid + r16) * 128u + (((4u * k + g) ^ ((r16 >> 1) & 7u)) * 16u));
#pragma unroll
    for (int p = 0; p < 2; ++p) c.vw[p] = (ldsp)(size_t)(ldsb + 32768u + g * 2048u + ((p ^ (g & 1u)) * 32u) + ((lane >> 2) & 3u) * 64u + (lane & 3u) * 8u);
    c.voff = lane * 16u; c.m0r = rfl(ldsb + wid * 4096u); c.sor = rfl(wid * 4096u);
    c.srdK = mk_srd(ws + WS_K + (size_t)h * HSTR, 524288u); c.srdV = mk_srd(ws + WS_V + (size_t)h * HSTR, 524288u);
    c.qdst = ldsb + (wid < 2 ? L_XA : L_XB) + (wid & 1u) * 16384u + lane * 16u;
    a_zero_n(make_iseq<8>{});
    asm volatile("s_waitcnt vmcnt(18)" ::: "memory");
    __builtin_amdgcn_s_barrier(); asm volatile("" ::: "memory");
        qp_read0(c, make_iseq<20>{});
    GAP();
    c.kst = ut < 0;
    qp_step<0, 0, 16>(c, 0u, Q_STG, 2 * Q_STG, 0u); qp_step<0, 0, 22>(c, Q_STG, 2 * Q_STG, 0u, 1u); qp_step<0, 0, 28>(c, 2 * Q_STG, 0u, Q_STG, 2u);
    unsigned st = 0, stp = 2 * Q_STG, fl = 0, flu = 0;
    for (unsigned kt = 3; kt < 7; ++kt) {
        const unsigned stn = st == 2 * Q_STG ? 0u : st + Q_STG;
        if (kt == 4 && tid < 4u) { if (sig1) sig_tag(sig1 + tid, ep); if (sig2 && tid == 0u) sig_tag(sig2, ep); }
        if (kt == 6) { fl = flag_issue(cnt_ptr(ws, C_KV + h)); if (ut >= 0) flu = flag_issue(cnt_ptr(ws, C_U + ut % 10)); }
        qp_step<0, 0>(c, st, stn, stp, kt); stp = st; st = stn;
    }
        asm volatile("" : "+v"(fl));
    unsigned rdy = kv_ready(fl, ep); kv_wait(rdy, 0u, cnt_ptr(ws, C_KV + h), ep);
    qp_step<1, 0>(c, Q_STG, 2 * Q_STG, 0u, 7u);
    qp_step<2, 1>(c, 2 * Q_STG, 0u, Q_STG, 8u);
        if (ut >= 0) { flag_check(flu, cnt_ptr(ws, C_U + ut % 10), 16u, ep); u_sload_all(uk, ws, ut); }
    qp_step<3, 2>(c, 0u, 0u, 2 * Q_STG, 9u);
    asm volatile("s_nop 7" ::: "memory");
    qp_store<12>(c); qp_store<13>(c); qp_store<14>(c); qp_store<15>(c);
        return rdy;
}

struct Lam { float ar, ai, cr, ci; };
DI Lam lam_of(unsigned rre, unsigned rim, unsigned rdt) {
    const float lr = bf2f((u16)rre), li = bf2f((u16)rim), dt = bf2f(f2bf(expf(bf2f((u16)rdt))));
    const float e = expf(dt * lr); float sn, cs; sincosf(dt * li, &sn, &cs);
    Lam L; L.ar = e * cs; L.ai = e * sn;
    const float nr = L.ar - 1.f, ni = L.ai, id = 2.f / (lr * lr + li * li);
    L.cr = (nr * lr + ni * li) * id; L.ci = (ni * lr - nr * li) * id;
    return L;
}
DI f32x2 cmul(f32x2 a, f32x2 b) { return (f32x2){a[0] * b[0] - a[1] * b[1], a[0] * b[1] + a[1] * b[0]}; }
DI f32x2 cfma(f32x2 a, f32x2 b, f32x2 c) { return (f32x2){fmaf(a[0], b[0], fmaf(-a[1], b[1], c[0])), fmaf(a[0], b[1], fmaf(a[1], b[0], c[1]))}; }
constexpr unsigned U_STG = 32768;
struct UC {
    ldsp px[2], py[2], px2[2], py2[2];
    unsigned vx[2], vy[2], m0x, m0y, sx, sy;
    __amdgpu_buffer_rsrc_t srdX, srdR, srdI;
    unsigned rre, rim, rdt;
    f32x4 ar[8], ai[8];
    s16x8 X[2][8], Y[2][2];
};
template <int T, int K32, int J> DI void u_read(UC& c) {
    constexpr int st = ((T >> 1) & 1) * U_STG; const ldsp bx = (T & 1) ? c.px2[K32] : c.px[K32], by = (T & 1) ? c.py2[K32] : c.py[K32];
    if constexpr (J < 2) c.Y[K32][J] = lds_b128(by + st + J * 8192); else c.X[K32][J - 2] = lds_b128(bx + st + (J - 2) * 2048);
}
template <int T, int P> DI void u_dma(UC& c) {
    constexpr unsigned st = ((T >> 1) & 1) * U_STG + (T & 1) * 65536u;
    if constexpr (T < 10) {
        if constexpr (P < 4) dma16(c.m0x + st + P * 1024, c.vx[P & 1], c.srdX, c.sx + T * 128u + ((P & 1) * 64 + (P >> 1) * 4) * 1280u);
        else dma16(c.m0y + st + ((P - 4) >> 1) * 8192 + (P & 1) * 1024, c.vy[P & 1], P < 6 ? c.srdR : c.srdI, c.sy + T * 128u + (P & 1) * 8u * 1280u);
    }
}
template <int T, int G> DI void u_gap(UC& c, const KvOut& o) {
    constexpr int S = G >> 4, I = G & 15;
    if constexpr (I & 1) mfma16v(c.ai[I >> 1], c.X[S][I >> 1], c.Y[S][1]); else mfma16v(c.ar[I >> 1], c.X[S][I >> 1], c.Y[S][0]);
    GAP();
    if constexpr (G == 16 && T < 9) { asm volatile("s_waitcnt vmcnt(%c0)" ::"i"((T + 2 < 10 ? 8 : 0) + (T + 3 < 10 ? 8 : 0) + 6) : "memory"); __builtin_amdgcn_s_waitcnt(0xC07F); __builtin_amdgcn_s_barrier(); asm volatile("" ::: "memory"); }
    if constexpr (I >= 1 && I <= 10) { if constexpr (S == 0) u_read<T, 1, I - 1>(c); else if constexpr (T < 9) u_read<T + 1, 0, I - 1>(c); }
    if constexpr (S == 1 && (I & 3) == 3) u_dma<T + 4, (I >> 2)>(c);
    if constexpr (S == 0 && (I & 3) == 3) u_dma<T + 3, 4 + (I >> 2)>(c);
    if constexpr (G == 6 || G == 13 || G == 28) kv_store<2 + 3 * T + (G == 6 ? 0 : G == 13 ? 1 : 2)>(o);
    GAP();
}
template <int T, int... Gs> DI void u_step(UC& c, const KvOut& o, iseq<Gs...>) { (u_gap<T, Gs>(c, o), ...); }
template <int... Ts> DI void u_steps(UC& c, const KvOut& o, iseq<Ts...>) { (u_step<Ts>(c, o, make_iseq<32>{}), ...); }
template <int T, int... Ps> DI void u_dma_all(UC& c, iseq<Ps...>) { (u_dma<T, Ps>(c), ...); }
template <int... Js> DI void u_read0(UC& c, iseq<Js...>) { (u_read<0, 0, Js>(c), ...); }
struct UKeep { f32x4 ar[8], ai[8]; f32x2 lam, cin, S[15]; f32x4 ex[4]; };
DI void u_pre(UC& c, char* lds, const u16* ax, const u16* bre, const u16* bim, const u16* lre, const u16* lim, const u16* logdt, int ut) {
    const unsigned tid = otid(), lane = tid & 63u, wid = rfl(tid >> 6), r16 = lane & 15u;
    const int ch = ut / 10, cg = ut % 10, p = 64 * cg + 16 * (int)wid + (int)r16;
    const unsigned ldsb = (unsigned)(size_t)lds, i8 = lane >> 3;
#pragma unroll
    for (int par = 0; par < 2; ++par) { const unsigned sw = ((lane & 7u) ^ ((4u * par + (i8 >> 1)) & 7u)) * 16u;
        c.vx[par] = (32u * (i8 >> 2) + (i8 & 3u)) * 1280u + sw; c.vy[par] = i8 * 1280u + sw; }
    c.m0x = rfl(ldsb + wid * 4096u); c.m0y = rfl(ldsb + 16384u + wid * 2048u);
    c.sx = rfl((unsigned)(128 * ch + 8 * (int)wid) * 1280u); c.sy = rfl((unsigned)(64 * cg + 16 * (int)wid) * 1280u);
    c.srdX = mk_srd(ax, 2048u * 1280u); c.srdR = mk_srd(bre, 640u * 1280u); c.srdI = mk_srd(bim, 640u * 1280u);
    asm volatile("s_nop 4\n\tglobal_load_ushort %0, %3, %4\n\tglobal_load_ushort %1, %3, %5\n\tglobal_load_ushort %2, %3, %6" : "=&v"(c.rre), "=&v"(c.rim), "=&v"(c.rdt) : "v"((unsigned)p * 2u), "s"(lre), "s"(lim), "s"(logdt) : "memory");
    u_dma_all<0>(c, make_iseq<8>{}); u_dma_all<1>(c, make_iseq<8>{});
}
template <class NEXT> DI void u_tile(UC& c, UKeep& k, const KvOut& o, char* lds, char* ws, int ut, unsigned* kvsig, unsigned ep, NEXT&& next) {
    const unsigned tid = otid(), lane = tid & 63u, wid = rfl(tid >> 6), r16 = lane & 15u, g = lane >> 4;
    const int ch = ut / 10, cg = ut % 10, p = 64 * cg + 16 * (int)wid + (int)r16;
    const unsigned ldsb = (unsigned)(size_t)lds;
    u_dma_all<2>(c, make_iseq<8>{}); u_dma_all<3>(c, make_iseq<4>{});
    kv_store_n<0>(o, make_iseq<2>{});
#pragma unroll
    for (int kk = 0; kk < 2; ++kk) { const unsigned sw = ((4u * kk + g) ^ ((r16 >> 1) & 7u)) * 16u;
        c.px[kk] = (ldsp)(size_t)(ldsb + r16 * 128u + sw); c.py[kk] = (ldsp)(size_t)(ldsb + 16384u + (16u * wid + r16) * 128u + sw); c.px2[kk] = c.px[kk] + 65536; c.py2[kk] = c.py[kk] + 65536; }
#pragma unroll
    for (int m = 0; m < 8; ++m) { c.ar[m] = (f32x4){0.f, 0.f, 0.f, 0.f}; c.ai[m] = (f32x4){0.f, 0.f, 0.f, 0.f}; asm volatile("" : "+v"(c.ar[m]), "+v"(c.ai[m])); }
    asm volatile("s_waitcnt vmcnt(14)\n\ts_barrier" : "+v"(c.rre), "+v"(c.rim), "+v"(c.rdt)::"memory");
        u_read0(c, make_iseq<10>{});
    GAP();
    u_steps(c, o, make_iseq<10>{});
    asm volatile("s_nop 7\n\ts_barrier" ::: "memory");
    GAP();
        next();
    const Lam L = lam_of(c.rre, c.rim, c.rdt);
    const f32x2 lam = {L.ar, L.ai}, cf = {L.cr, L.ci};
    f32x2 E = {0.f, 0.f};
#pragma unroll
    for (int m = 0; m < 8; ++m)
#pragma unroll
        for (int e = 0; e < 4; ++e) { const f32x2 u = cmul(cf, (f32x2){c.ar[m][e], c.ai[m][e]}); k.ar[m][e] = u[0]; k.ai[m][e] = u[1]; E = cfma(lam, E, u); }
    f32x2 l32 = lam;
#pragma unroll
    for (int i = 0; i < 5; ++i) l32 = cmul(l32, l32);
    f32x2 cin = {0.f, 0.f};
#pragma unroll
    for (int j = 0; j < 3; ++j) { const f32x2 Ej = {__shfl(E[0], (int)r16 + 16 * j), __shfl(E[1], (int)r16 + 16 * j)}; const f32x2 nx = cfma(l32, cin, Ej); if ((int)g > j) cin = nx; }
    k.lam = lam; k.cin = cin;
    const __amdgpu_buffer_rsrc_t srdS = mk_srd(ws + WS_SEND, 16u * 5120u);
    if (g == 3) __builtin_amdgcn_raw_buffer_store_b64(__builtin_bit_cast(u32x2, cfma(l32, cin, E)), srdS, (unsigned)p * 8u, (unsigned)ch * 5120u, 16);
    asm volatile("s_waitcnt vmcnt(13)" ::: "memory");
    if (lane == 0) sig_tag(kvsig + wid, ep);
}
template <int J> DI void u_sload1(UKeep& k, unsigned voff, __amdgpu_buffer_rsrc_t srd) { asm volatile("buffer_load_dwordx2 %0, %1, %2, %3 offen sc1" : "=v"(k.S[J]) : "v"(voff), "s"(srd), "s"(J * 5120) : "memory"); }
template <int... Js> DI void u_sload(UKeep& k, char* ws, int ut, iseq<Js...>) {
    const unsigned p = 64u * (unsigned)(ut % 10) + (otid() >> 6) * 16u + (otid() & 15u);
    const __amdgpu_buffer_rsrc_t srdS = mk_srd(ws + WS_SEND, 16u * 5120u);
    asm volatile("s_nop 4" ::: "memory");
    (u_sload1<Js>(k, p * 8u, srdS), ...);
}
template <class UK> DI void u_sload_all(UK& k, char* ws, int ut) { u_sload(k, ws, ut, make_iseq<15>{}); }
DI void u_fin(UKeep& k, char* lds, char* ws, int ut) {
        const unsigned tid = otid(), lane = tid & 63u, wid = rfl(tid >> 6), r16 = lane & 15u, g = lane >> 4;
    const int ch = ut / 10, cg = ut % 10;
    const unsigned scr = (unsigned)(size_t)lds + 32768u + wid * 4096u;
    asm volatile("s_waitcnt vmcnt(0)" : "+v"(k.S[0]), "+v"(k.S[1]), "+v"(k.S[2]), "+v"(k.S[3]), "+v"(k.S[4]), "+v"(k.S[5]), "+v"(k.S[6]), "+v"(k.S[7]), "+v"(k.S[8]), "+v"(k.S[9]), "+v"(k.S[10]), "+v"(k.S[11]),
                 "+v"(k.S[12]), "+v"(k.S[13]), "+v"(k.S[14])::"memory");
        const f32x2 lam = k.lam;
    f32x2 l32 = lam;
#pragma unroll
    for (int i = 0; i < 5; ++i) l32 = cmul(l32, l32);
    const f32x2 l64 = cmul(l32, l32), l128 = cmul(l64, l64);
    f32x2 pw = (g & 1u) ? l32 : (f32x2){1.f, 0.f};
    if (g & 2u) pw = cmul(pw, l64);
    f32x2 H = {0.f, 0.f};
#pragma unroll
    for (int j = 0; j < 15; ++j) { const f32x2 nx = cfma(l128, H, k.S[j]); if (j < ch) H = nx; }
    f32x2 h = cfma(pw, H, k.cin);
    const __amdgpu_buffer_rsrc_t srdH = mk_srd(ws + WS_HT + (size_t)ch * HT_S, 327680u);
    const unsigned sw = scr + r16 * 256u, wx = (r16 & 7u) * 32u, rq = (lane >> 2) & 3u, sr = scr + (8u * (g & 1u) + rq) * 256u + (lane & 3u) * 8u;
    const unsigned vo = (lane >> 5) * 2048u + (wid >> 1) * 1024u + (wid & 1u) * 512u + (lane & 31u) * 16u;
    u32x4 wi[4];
#pragma unroll
    for (int q = 0; q < 4; ++q) {
        u32x4 wr;
#pragma unroll
        for (int m2 = 0; m2 < 2; ++m2) { const int m = 2 * q + m2; f32x2 hh[4];
#pragma unroll
            for (int e = 0; e < 4; ++e) { h = cfma(lam, h, (f32x2){k.ar[m][e], k.ai[m][e]}); hh[e] = h; }
            wr[2 * m2] = cvtpk(hh[0][0], hh[1][0]); wr[2 * m2 + 1] = cvtpk(hh[2][0], hh[3][0]);
            wi[q][2 * m2] = cvtpk(-hh[0][1], -hh[1][1]); wi[q][2 * m2 + 1] = cvtpk(-hh[2][1], -hh[3][1]); }
        *(LAS u32x4*)(size_t)(sw + ((g * 64u + q * 16u) ^ wx)) = wr;
    }
#pragma unroll
    for (int part = 0; part < 2; ++part) {
        s16x8 f[4];
#pragma unroll
        for (int i = 0; i < 4; ++i) f[i] = cat8(lds_tr((ldsp)(size_t)(sr + ((i * 64u + (lane >> 5) * 32u) ^ (rq * 32u)))), lds_tr((ldsp)(size_t)(sr + 1024u + ((i * 64u + (lane >> 5) * 32u) ^ (rq * 32u + 128u)))));
        __builtin_amdgcn_s_waitcnt(0xC07F); asm volatile("" ::: "memory");
        if (part == 0) {
#pragma unroll
            for (int q = 0; q < 4; ++q) *(LAS u32x4*)(size_t)(sw + ((g * 64u + q * 16u) ^ wx)) = wi[q];
        }
#pragma unroll
        for (int i = 0; i < 4; ++i) st128(__builtin_bit_cast(u32x4, f[i]), srdH, vo, (unsigned)(10 * part + cg) * 16384u + i * 4096u);
    }
}
constexpr unsigned WB_(unsigned kk) { return kk < 3u ? kk * 10240u : kk < 6u ? 65536u + (kk - 3u) * 10240u : 135168u + (kk - 6u) * 10240u; }
DI void wo_pre(char* lds, const u16* wo, int z) {
    const unsigned tid = otid(), lane = tid & 63u, wid = rfl(tid >> 6), hi = lane >> 5, row = (lane >> 2) & 7u, ldsb = (unsigned)(size_t)lds;
    const __amdgpu_buffer_rsrc_t srd = mk_srd((const char*)wo + (size_t)(z & 63) * 81920, z < 64 ? 81920u : 0u);
    const unsigned vs = row * 1280u + hi * 64u + (((lane & 3u) ^ ((row >> 2) * 2u)) * 16u), so = rfl(wid * 20480u), m0 = rfl(ldsb + WB_(2u * wid)), m1 = rfl(ldsb + WB_(2u * wid + 1u));
    asm volatile("s_nop 4" ::: "memory");
#pragma unroll
    for (unsigned p = 0; p < 20; ++p) dma16nt((p < 10u ? m0 : m1) + (p % 10u) * 1024u, vs, srd, so + (p / 10u) * 10240u + (p % 10u) * 128u);
}
template <class NEXT> DI void wo_tile(UKeep& k, const KvOut& o, char* lds, NEXT&& next) {
    const unsigned tid = otid(), lane = tid & 63u, wid = rfl(tid >> 6), g = lane >> 4, ldsb = (unsigned)(size_t)lds;
    kv_store_n<0>(o, make_iseq<16>{});
    asm volatile("s_waitcnt vmcnt(16)\n\ts_barrier" ::: "memory");
    const unsigned lo = ldsb + wid * 2560u + (4u * (g & 1u) + ((lane >> 2) & 3u)) * 64u + (lane & 3u) * 8u;
#pragma unroll
    for (int i = 0; i < 20; ++i) { const int k32 = i / 10, cb = i % 10; const unsigned o2 = lo + (cb >> 1) * 512 + (((cb & 1) ^ (g & 1u)) * 32u);
        const f32x4 f = __builtin_bit_cast(f32x4, cat8(lds_tr((ldsp)(size_t)(o2 + ((g >> 1) ? WB_(4 * k32 + 1) : WB_(4 * k32)))), lds_tr((ldsp)(size_t)(o2 + ((g >> 1) ? WB_(4 * k32 + 3) : WB_(4 * k32 + 2))))));
        if (i < 8) k.ar[i] = f; else if (i < 16) k.ai[i - 8] = f; else k.ex[i - 16] = f; }
    __builtin_amdgcn_s_waitcnt(0xC07F); __builtin_amdgcn_s_barrier(); asm volatile("" ::: "memory");
    next();
}
DI void wo_fin(UKeep& k, char* ws, int z) {
    const unsigned tid = otid(), lane = tid & 63u, wid = rfl(tid >> 6);
    const __amdgpu_buffer_rsrc_t srdT = mk_srd(ws + WS_WOT + (size_t)((z & 63) * 4 + (int)wid) * 20480, z < 64 ? 20480u : 0u);
#pragma unroll
    for (int i = 0; i < 20; ++i) st128(__builtin_bit_cast(u32x4, i < 8 ? k.ar[i] : i < 16 ? k.ai[i - 8] : k.ex[i - 16]), srdT, lane * 16u, i * 1024);
}

constexpr int SCH_A[68] = {0,0,1,2,3,4,33,73,49,0,65,5,50,6,7,8,58,66,9,10,36,51,11,12,67,59,13,14,15,52,16,68,17,60,18,19,20,41,69,53,21,22,61,23,24,70,54,25,26,27,62,28,71,29,30,55,31,32,47,72,63,0,0,56,0,0,0,64};
constexpr int SCH_B[68] = {0,0,0,0,0,0,34,0,57,0,0,0,0,0,0,0,35,0,0,0,37,0,0,0,0,38,0,0,0,39,0,0,0,40,0,0,0,0,0,42,0,0,43,0,0,0,44,0,0,0,45,0,0,0,0,46,0,0,48,0,0,0,0,0,0,0,0,0};

enum { F_QK = 1, F_CZ = 2, F_SM = 4, F_MASK = 8, F_PV = 16, F_RK = 32, F_RV = 64, F_DK = 128, F_DV = 256, F_BAR = 512, F_QR = 1024, F_MX = 2048, F_SEND = 4096, F_RECV = 8192 };
DI unsigned* out_flag(unsigned& need);
struct AttnC {
    ldsp vk, vva, vvb;
    ldsp kp, vpa, vpb;
    unsigned voff;
    unsigned m0r;
    unsigned s0, s1, s2;
    unsigned t0, t1, t2;
    unsigned ksrc, vsrc, kend, vend, kwrap, vwrap;
    unsigned qst;
    unsigned xw, xr;
    __amdgpu_buffer_rsrc_t srdK, srdV;
    f32x4 S[2][2][4];
    f32x4 NEGM[4];
    u32x4 P[2][4];
    s16x8 Kf[8], Vf[8];
    f32x4 mo, lo, mx;
    ldsp stw, str;
    f32x4 so, sp;
    f32x2 st[4];
    f32x4 rx[2];
    __amdgpu_buffer_rsrc_t srdO;
    int dm;
    bool mk[5][4];
    bool bad;
};
constexpr int AO_(int f, int qb) { return 4 * (4 * f + qb); }
constexpr int AL_(int qb) { return 128 + 4 * qb; }
constexpr int AQ_(int qb, int ds) { return 144 + 16 * qb + 4 * ds; }
constexpr int AONES = 208, AZERO = 212;

template <int B, bool CZ, int F, int QB> DI void at_qk(AttnC& c) {
    constexpr int kb = F >> 2, ds = F & 3, aq = AQ_(QB, ds);
    if constexpr (ds == 0) {
        if constexpr (CZ) asm volatile("v_mfma_f32_16x16x32_bf16 %0, %1, a[%c2:%c3], 0" : "=&v"(c.S[B][kb][QB]) : "v"(c.Kf[F]), "i"(aq), "i"(aq + 3));
        else asm volatile("v_mfma_f32_16x16x32_bf16 %0, %1, a[%c3:%c4], %2" : "=&v"(c.S[B][kb][QB]) : "v"(c.Kf[F]), "v"(c.NEGM[QB]), "i"(aq), "i"(aq + 3));
    } else asm volatile("v_mfma_f32_16x16x32_bf16 %0, %1, a[%c2:%c3], %0" : "+v"(c.S[B][kb][QB]) : "v"(c.Kf[F]), "i"(aq), "i"(aq + 3));
}
template <int B, int DB, int QB> DI void at_pv(AttnC& c) {
    constexpr int ao = AO_(DB, QB);
    asm volatile("v_mfma_f32_16x16x32_bf16 a[%c2:%c3], %0, %1, a[%c2:%c3]" ::"v"(c.Vf[DB]), "v"(c.P[B][QB]), "i"(ao), "i"(ao + 3));
}
template <int B, int QB> DI void at_ls(AttnC& c) {
    constexpr int al = AL_(QB);
    asm volatile("v_mfma_f32_16x16x32_bf16 a[%c1:%c2], a[%c3:%c4], %0, a[%c1:%c2]" ::"v"(c.P[B][QB]), "i"(al), "i"(al + 3), "i"(AONES), "i"(AONES + 3));
}
template <int I> DI void a_zero16() { asm volatile("v_mfma_f32_32x32x16_bf16 a[%c0:%c1], a[%c2:%c3], a[%c2:%c3], 0" ::"i"(16 * I), "i"(16 * I + 15), "i"(AZERO), "i"(AZERO + 3)); }
DI float exp2_s(float x) { return __builtin_amdgcn_exp2f(x); }
template <int QB, int DS> DI void q_read(unsigned addr) {
    asm volatile("ds_read_b128 a[%c1:%c2], %0 offset:%c3" ::"v"(addr), "i"(AQ_(QB, DS)), "i"(AQ_(QB, DS) + 3), "i"(QB * 4096 + DS * 1024) : "memory");
}
template <int... Is> DI void q_read_all(unsigned addr, iseq<Is...>) { (q_read<(Is >> 2), (Is & 3)>(addr), ...); }
template <int T> DI void o_send(unsigned addr) {
    asm volatile("ds_write_b128 %0, a[%c1:%c2] offset:%c3" ::"v"(addr), "i"(64 + 4 * T), "i"(67 + 4 * T), "i"(T * 1024) : "memory");
}
DI float max3f(float a, float b, float c) { float r; asm("v_max3_f32 %0, %1, %2, %3" : "=v"(r) : "v"(a), "v"(b), "v"(c)); return r; }
DI float max2f(float a, float b) { float r; asm("v_max_f32_e32 %0, %1, %2" : "=v"(r) : "v"(a), "v"(b)); return r; }

template <int B, int FL, int VM, int OP> DI void at_op(AttnC& c) {
    if constexpr (OP >= 1 && OP <= 32) {
        if constexpr (FL & F_SM) { constexpr int e4 = OP - 1, t = e4 >> 2, e = e4 & 3, kb = t >> 2, qb = t & 3;
            float x = c.S[B][kb][qb][e];
            if constexpr (FL & F_MASK) x = c.mk[kb - qb + 3][e] ? -__builtin_inff() : x;
            c.S[B][kb][qb][e] = exp2_s(x); }
    } else if constexpr (OP >= 33 && OP <= 48) {
        if constexpr (FL & F_SM) { constexpr int ci = OP - 33, t = ci >> 1, h = ci & 1, kb = t >> 2, qb = t & 3;
            c.P[B][qb][2 * kb + h] = cvtpk(c.S[B][kb][qb][2 * h], c.S[B][kb][qb][2 * h + 1]); }
    } else if constexpr (OP >= 49 && OP <= 56) {
        if constexpr (FL & F_RK) c.Kf[OP - 49] = lds_b128(c.kp + (OP - 49) * 1024);
    } else if constexpr (OP >= 57 && OP <= 64) {
        if constexpr (FL & F_RV) c.Vf[OP - 57] = lds_b128((OP - 57 < 4 ? c.vpa : c.vpb) + (OP - 57) * 1024);
    } else if constexpr (OP >= 65 && OP <= 68) {
        if constexpr (FL & F_DK) dma4<OP - 65>(c.m0r + c.s2, c.voff, c.srdK, c.ksrc);
    } else if constexpr (OP >= 69 && OP <= 72) {
        if constexpr (FL & F_DV) dma4<OP - 69>(c.m0r + c.t2, c.voff, c.srdV, c.vsrc);
    } else if constexpr (OP == 73) {
        if constexpr (FL & F_BAR) { asm volatile("s_waitcnt vmcnt(%c0)" ::"i"(VM) : "memory"); __builtin_amdgcn_s_waitcnt(0xC07F); __builtin_amdgcn_s_barrier(); asm volatile("" ::: "memory"); }
    }
}
template <int QB, int K> DI void at_mx(AttnC& c) {
    if constexpr (K < 2) {
    } else if constexpr (K == 2) {
        const float a = max3f(c.S[0][0][QB][0], c.S[0][0][QB][1], c.S[0][0][QB][2]), b = max3f(c.S[0][0][QB][3], c.S[0][1][QB][0], c.S[0][1][QB][1]);
        c.mx[QB] = max3f(max3f(a, c.S[0][1][QB][2], c.S[0][1][QB][3]), b, -1e30f);
    } else if constexpr (K == 3) {
        auto r = __builtin_amdgcn_permlane16_swap(__float_as_uint(c.mx[QB]), __float_as_uint(c.mx[QB]), false, false); c.mx[QB] = max2f(__uint_as_float(r[0]), __uint_as_float(r[1]));
    } else if constexpr (K == 4) {
        auto r = __builtin_amdgcn_permlane32_swap(__float_as_uint(c.mx[QB]), __float_as_uint(c.mx[QB]), false, false); c.mx[QB] = -max2f(__uint_as_float(r[0]), __uint_as_float(r[1]));
    } else if constexpr (K == 5) {
        c.NEGM[QB] = (f32x4){c.mx[QB], c.mx[QB], c.mx[QB], c.mx[QB]}; asm volatile("" : "+v"(c.NEGM[QB]));
    } else if constexpr (K == 6) { c.S[0][0][QB] += c.mx[QB]; asm volatile("" : "+v"(c.S[0][0][QB]));
    } else if constexpr (K == 7) { c.S[0][1][QB] += c.mx[QB]; asm volatile("" : "+v"(c.S[0][1][QB])); }
}

template <int QB> DI void at_stat_send(AttnC& c) {
    const float l = a_read<AL_(QB)>();
    c.lo[QB] = l;
    *(LAS f32x2*)(c.stw + c.t2 + QB * 128) = (f32x2){c.mo[QB], l};
}
template <int QB> DI void at_stat_rd(AttnC& c) { c.st[QB] = *(const LAS f32x2*)(c.str + c.t2 + QB * 128); }
template <int QB> DI void at_stat_recv(AttnC& c) {
    const f32x2 o = c.st[QB];
    const float M = max2f(c.mo[QB], o[0]), wo = exp2_s(c.mo[QB] - M), wp = exp2_s(o[0] - M), il = __builtin_amdgcn_rcpf(c.lo[QB] * wo + o[1] * wp);
    c.so[QB] = wo * il; c.sp[QB] = wp * il;
    c.bad |= !(c.lo[QB] < 1.8e19f) | !(o[1] < 1.8e19f);
}
template <int J> DI void at_rx_read(AttnC& c) {
    constexpr int qb = J >> 1, dsl = J & 1;
    c.rx[0] = *(const LAS f32x4*)(size_t)(c.xr + (8 * dsl + qb) * 1024); c.rx[1] = *(const LAS f32x4*)(size_t)(c.xr + (8 * dsl + 4 + qb) * 1024);
}
template <int J> DI void at_rx_fin(AttnC& c, const f32x4 r0, const f32x4 r1) {
    constexpr int qb = J >> 1, dsl = J & 1;
    const f32x4 o0 = a_read4<AO_(2 * dsl, qb)>() * c.so[qb] + r0 * c.sp[qb];
    const f32x4 o1 = a_read4<AO_(2 * dsl + 1, qb)>() * c.so[qb] + r1 * c.sp[qb];
    u32x4 w; w[0] = cvtpk(o0[0], o0[1]); w[1] = cvtpk(o0[2], o0[3]); w[2] = cvtpk(o1[0], o1[1]); w[3] = cvtpk(o1[2], o1[3]);
    st128(w, c.srdO, c.voff, qb * 2048 + dsl * 1024);
}
template <int B, int FL, int VM, int G> DI void at_gap(AttnC& c) {
    constexpr int pr = G / 17, w = G % 17, f = 2 * pr + (w >= 8 ? 1 : 0), i = w == 16 ? 8 : (w & 7);
    if constexpr (G == 0 && (FL & F_CZ) && (FL & F_PV)) asm volatile("s_waitcnt lgkmcnt(15)" ::: "memory");
    if constexpr (i == 8) { if constexpr (FL & F_PV) at_ls<B ^ 1, pr>(c); }
    else if constexpr (i < 4) { if constexpr (FL & F_QK) at_qk<B ^ 1, (FL & F_CZ) != 0, f, i>(c); }
    else { if constexpr (FL & F_PV) at_pv<B ^ 1, f, i - 4>(c); }
    GAP();
    if constexpr (G == 0 && (FL & F_RK)) { c.kp = c.vk + c.s0; asm volatile("" : "+v"(c.kp)); }
    if constexpr (G == 1 && (FL & F_RV)) { c.vpa = c.vva + c.t0; c.vpb = c.vvb + c.t0; asm volatile("" : "+v"(c.vpa), "+v"(c.vpb)); }
    if constexpr (FL & F_MASK) {
        constexpr int di = G == 0 ? 3 : G == 9 ? 2 : G == 12 ? 1 : G == 21 ? 0 : G == 29 ? 4 : -1;
        if constexpr (di >= 0) {
#pragma unroll
            for (int e = 0; e < 4; ++e) c.mk[di][e] = c.dm > -(16 * (di - 3) + e);
            GAP();
        }
    }
    if constexpr (G == 9 || G == 61) ACC_GUARD();
    at_op<B, FL, VM, SCH_A[G]>(c);
    at_op<B, FL, VM, SCH_B[G]>(c);
    if constexpr (FL & F_QR) {
        if constexpr (G >= 38 && G < 42) q_read<G - 38, 0>(c.qst);
        if constexpr (G >= 46 && G < 50) q_read<G - 46, 1>(c.qst);
        if constexpr (G >= 55 && G < 59) q_read<G - 55, 2>(c.qst);
        if constexpr (G >= 63 && G < 67) q_read<G - 63, 3>(c.qst);
    }
    if constexpr (FL & F_MX) { if constexpr (i >= 4) { constexpr int k = w == 16 ? 8 : (w >= 8 ? w - 8 : w - 4); if constexpr (k < 8) at_mx<pr, k>(c); } }
    if constexpr (FL & F_SEND) {
        if constexpr (G == 46 || G == 55 || G == 63) asm volatile("s_nop 3" ::: "memory");
        if constexpr (G >= 46 && G < 50) o_send<G - 46>(c.xw);
        if constexpr (G >= 55 && G < 59) o_send<4 + G - 55>(c.xw);
        if constexpr (G >= 63 && G < 67) o_send<8 + G - 63>(c.xw);
        if constexpr (G == 30) at_stat_send<0>(c);
        if constexpr (G == 47) at_stat_send<1>(c);
        if constexpr (G == 64) at_stat_send<2>(c);
    }
    if constexpr (FL & F_RECV) {
        if constexpr (G == 3) { asm volatile("s_nop 3" ::: "memory"); o_send<12>(c.xw); o_send<13>(c.xw); o_send<14>(c.xw); o_send<15>(c.xw); at_stat_send<3>(c); }
        if constexpr (G == 7) { at_stat_rd<0>(c); at_stat_rd<1>(c); at_stat_rd<2>(c); at_stat_rd<3>(c); at_rx_read<0>(c); }
        if constexpr (G == 11 || G == 17 || G == 25 || G == 34) at_stat_recv<(G == 11 ? 0 : G == 17 ? 1 : G == 25 ? 2 : 3)>(c);
        if constexpr (G >= 12 && G < 68 && ((G - 12) % 7) == 0) { constexpr int J = (G - 12) / 7; const f32x4 r0 = c.rx[0], r1 = c.rx[1]; if constexpr (J < 7) at_rx_read<J + 1>(c); at_rx_fin<J>(c, r0, r1); }
        if constexpr (G == 12) { a_zero16<4>(); a_zero16<5>(); }
        if constexpr (G == 13) { a_zero16<6>(); a_zero16<7>(); }
        if constexpr (G == 14) a_zero16<8>();
        if constexpr (G == 67) { asm volatile("s_nop 1" ::: "memory"); a_zero16<0>(); a_zero16<1>(); a_zero16<2>(); a_zero16<3>(); }
    }
    GAP();
}
template <int B, int FL, int VM, int... Gs> DI void at_period_(AttnC& c, iseq<Gs...>) { (at_gap<B, FL, VM, Gs>(c), ...); }
template <int B, int FL, int VM = 8> DI void at_period(AttnC& c) {
    if constexpr (FL & F_MASK) asm volatile("" : "+v"(c.dm));
    at_period_<B, FL, VM>(c, make_iseq<68>{});
    if constexpr (FL & F_SM) asm volatile("" : "+v"(c.P[B][0]), "+v"(c.P[B][1]), "+v"(c.P[B][2]), "+v"(c.P[B][3]));
    if constexpr (FL & F_RK) { const unsigned t = c.s0; c.s0 = c.s1; c.s1 = c.s2; c.s2 = t; }
    if constexpr (FL & F_RV) { const unsigned t = c.t0; c.t0 = c.t1; c.t1 = c.t2; c.t2 = t; }
    if constexpr (FL & F_DK) { c.ksrc += 16384u; c.ksrc = c.ksrc >= c.kend ? c.kwrap : c.ksrc; }
    if constexpr (FL & F_DV) { c.vsrc += 16384u; c.vsrc = c.vsrc >= c.vend ? c.vwrap : c.vsrc; }
}
constexpr int F_IO = F_RK | F_RV | F_DK | F_DV | F_BAR, F_FULL = F_QK | F_SM | F_PV | F_IO;
template <int B> DI void at_fence(AttnC& c) {
    GAP(); asm volatile("s_nop 7" : "+v"(c.S[B][0][0]), "+v"(c.S[B][0][1]), "+v"(c.S[B][0][2]), "+v"(c.S[B][0][3]), "+v"(c.S[B][1][0]), "+v"(c.S[B][1][1]), "+v"(c.S[B][1][2]), "+v"(c.S[B][1][3])); GAP();
}
DI void at_mask0(AttnC& c, int dm) {
    asm volatile("" : "+v"(dm));
#pragma unroll
    for (int kb = 0; kb < 2; ++kb)
#pragma unroll
        for (int qb = 0; qb < 4; ++qb)
#pragma unroll
            for (int e = 0; e < 4; ++e) c.S[0][kb][qb][e] = (dm > -(16 * (kb - qb) + e)) ? -__builtin_inff() : c.S[0][kb][qb][e];
}
template <int... Ks> DI void at_mx_all(AttnC& c, iseq<Ks...>) { (at_mx<(Ks & 3), (Ks >> 2)>(c), ...); }
template <int J> DI void at_recv1(AttnC& c) { const f32x4 r0 = c.rx[0], r1 = c.rx[1]; if constexpr (J < 7) at_rx_read<J + 1>(c); at_rx_fin<J>(c, r0, r1); GAP(); }
template <int... Js> DI void at_recv_all(AttnC& c, iseq<Js...>) { (at_recv1<Js>(c), ...); }

DI unsigned attn_phase(char* lds, char* ws, int h, int qA, int qB, unsigned rdy, unsigned* hsig, unsigned hstr, unsigned ep) {
    const unsigned tid = otid(), lane = tid & 63u, wid = rfl(tid >> 6), r = wid & 1u, kg = wid >> 1, g = lane >> 4;
    const int nA = 2 * (qA + 1), nB = 2 * (qB + 1);
    const unsigned ldsb = (unsigned)(size_t)lds, sow = wid * 4096u;
    AttnC c;
        c.voff = lane * 16u;
    c.srdK = mk_srd(ws + WS_K + (size_t)h * HSTR, 524288u); c.srdV = mk_srd(ws + WS_V + (size_t)h * HSTR, 524288u);
    c.m0r = rfl(ldsb + sow);
    c.vk = (ldsp)(size_t)(ldsb + kg * 8192u + lane * 16u);
    c.vva = (ldsp)(size_t)(ldsb + kg * 8192u + lane * 16u + kg * 4096u); c.vvb = (ldsp)(size_t)(ldsb + kg * 8192u + lane * 16u - kg * 4096u);
    c.qst = ldsb + L_XB + r * 16384u + lane * 16u;
    c.xw = ldsb + (kg ? L_XB : L_XA) + r * 16384u + lane * 16u; c.xr = ldsb + (kg ? L_XA : L_XB) + r * 16384u + lane * 16u;
    c.stw = (ldsp)(size_t)(ldsb + (wid ^ 2u) * 4096u + (lane & 15u) * 8u); c.str = (ldsp)(size_t)(ldsb + wid * 4096u + (lane & 15u) * 8u);
    const int dmask = (int)(4u * g) - (int)(lane & 15u) + 32 * (int)kg - 64 * (int)r;
    const int sgo = C_CTX + (h / 11) * 2 + (h & 1);
    const unsigned sgw = (unsigned)((h % 11) >> 1) * 4u + wid;
#define sgA (cnt_ptr(ws, sgo + qA * 6) + sgw)
#define sgB (cnt_ptr(ws, sgo + qB * 6) + sgw)
    unsigned badm = 0; bool pend = false;
    asm volatile("v_accvgpr_write_b32 a208, %0\n\tv_accvgpr_write_b32 a209, %0\n\tv_accvgpr_write_b32 a210, %0\n\tv_accvgpr_write_b32 a211, %0\n\t"
                 "v_accvgpr_write_b32 a212, 0\n\tv_accvgpr_write_b32 a213, 0\n\tv_accvgpr_write_b32 a214, 0\n\tv_accvgpr_write_b32 a215, 0\n\ts_nop 1" ::"v"(0x3f803f80u));
    a_zero16<0>(); a_zero16<1>(); a_zero16<2>(); a_zero16<3>(); a_zero16<4>(); a_zero16<5>(); a_zero16<6>(); a_zero16<7>(); a_zero16<8>();
    c.s0 = L_K1; c.s1 = L_K2; c.s2 = L_K0; c.t0 = L_V0; c.t1 = L_V1; c.t2 = L_V2;
    c.kend = c.vend = sow + (unsigned)(nA > 2 ? nA : nB) * 16384u; c.kwrap = c.vwrap = nA > 2 ? sow : 0x10000000u;
    c.ksrc = sow + (nA > 2 ? 3u : 1u) * 16384u; c.vsrc = sow + (nA > 2 ? 2u : 0u) * 16384u;
    unsigned flv = 0;
    auto ensure = [&](bool first) {
        if (__builtin_expect(rdy < 8u, 0)) {
            unsigned* const kvf = cnt_ptr(ws, C_KV + h);
            if (!first) { asm volatile("s_waitcnt vmcnt(8)" : "+v"(flv)::"memory"); const unsigned nr = kv_ready(flv, ep); rdy = nr > rdy ? nr : rdy; }
            if (c.ksrc < 0x10000000u) { const unsigned t = ((c.ksrc - sow) >> 14) + 1u; kv_wait(rdy, (t > 31u ? 31u : t) >> 2, kvf, ep); }
            flv = flag_issue(kvf);
        } };
    asm volatile("s_waitcnt vmcnt(16)" ::: "memory"); __builtin_amdgcn_s_waitcnt(0xC07F); __builtin_amdgcn_s_barrier(); asm volatile("" ::: "memory");
        q_read_all(ldsb + L_XA + r * 16384u + lane * 16u, make_iseq<16>{});
#pragma unroll
    for (int f = 0; f < 8; ++f) c.Kf[f] = lds_b128(c.vk + L_K0 + f * 1024);
    asm volatile("s_waitcnt lgkmcnt(0)" ::: "memory");
    GAP();
    if (nA == 2) { c.s1 = L_K0; c.s2 = L_K2; }
    ensure(true);
    at_period<1, F_QK | F_CZ | F_RK | F_DK | F_BAR, 12>(c);
    at_fence<0>(c);
    if (__builtin_expect(nA == 2, 0)) at_mask0(c, dmask);
    at_mx_all(c, make_iseq<32>{});
    asm volatile("s_nop 1" ::: "memory");
    GAP();
    { const unsigned qb_ = c.qst; if (nA != 2) c.qst = ldsb + L_XA + r * 16384u + lane * 16u;
      at_period<0, F_QK | F_SM | F_IO | F_QR, 8>(c); at_fence<1>(c); c.qst = qb_; }
    for (int pass = 0; pass < 2; ++pass) {
        const int n = pass ? nB : nA;
                __builtin_amdgcn_s_waitcnt(0xC07F);
        for (int p = 1; p < n - 3; p += 2) {
            ensure(false);
            at_period<1, F_FULL>(c); at_period<0, F_FULL>(c);
            if (__builtin_expect(pend, 0)) { pend = false; if (lane == 0) sig_tag(sgA, ep); }
        }
                if (n > 2) { ensure(false); at_period<1, F_FULL>(c); c.dm = dmask; at_period<0, F_FULL | F_MASK | F_QR>(c); }
        c.dm = dmask + 64;
#pragma unroll
        for (int q = 0; q < 4; ++q) c.mo[q] = -c.NEGM[q][0];
        c.srdO = mk_srd(ws + WS_CTX + (size_t)(pass ? qB : qA) * CT_S + (2 * h + kg) * 16384u + r * 8192u, 8192u);
        c.bad = false;
        if (pass == 0) {
            c.kend = c.vend = sow + (unsigned)nB * 16384u; c.kwrap = c.vwrap = 0x10000000u; c.ksrc = c.ksrc >= c.kend ? c.kwrap : c.ksrc;
            ensure(false);
            at_period<1, F_FULL | F_CZ | F_MASK>(c);
                        at_fence<0>(c);
            if (hsig != nullptr && lane == 0 && (hstr || wid == 0)) sig_tag(hsig + wid, ep);
            if (__builtin_expect(nB == 2, 0)) at_mask0(c, dmask);
            GAP();
            at_period<0, F_PV | F_BAR | F_MX | F_SEND>(c);
                        at_period<0, F_QK | F_SM | F_IO | F_RECV>(c);
            at_fence<1>(c);
            if (__any(c.bad)) badm |= 1u; else pend = true;
        } else {
            unsigned need, fl = flag_issue(out_flag(need)), fw = flag_issue(cnt_ptr(ws, C_WO));
            at_period<1, F_SM | F_MASK | F_PV | F_RV | F_BAR>(c);
                        asm volatile("s_nop 1" ::: "memory");
            at_period<0, F_PV | F_BAR | F_SEND>(c);
                        asm volatile("s_nop 7" ::: "memory");
            o_send<12>(c.xw); o_send<13>(c.xw); o_send<14>(c.xw); o_send<15>(c.xw); at_stat_send<3>(c);
            asm volatile("s_waitcnt vmcnt(0)" ::: "memory");
            if (pend) { pend = false; if (lane == 0) sig_tag(sgA, ep); }
            asm volatile("" : "+v"(fl), "+v"(fw));
            if (!tags_ok(fl, need, ep) || !tags_ok(fw, 64u, ep)) badm |= 4u;
            __builtin_amdgcn_s_waitcnt(0xC07F); __builtin_amdgcn_s_barrier(); asm volatile("" ::: "memory");
            at_stat_rd<0>(c); at_stat_rd<1>(c); at_stat_rd<2>(c); at_stat_rd<3>(c); at_rx_read<0>(c);
            at_stat_recv<0>(c); at_stat_recv<1>(c); at_stat_recv<2>(c); at_stat_recv<3>(c);
            at_recv_all(c, make_iseq<8>{});
        }
    }
        asm volatile("s_waitcnt vmcnt(0)" ::: "memory");
    if (__any(c.bad)) badm |= 2u; else if (lane == 0) sig_tag(sgB, ep);
            return badm;
}
DI u16 ld16_dev(const u16* p) { return __hip_atomic_load(p, __ATOMIC_RELAXED, __HIP_MEMORY_SCOPE_AGENT); }
DI float kq_el(const char* img, int tok, int d) { return bf2f(ld16_dev((const u16*)(img + (tok >> 4) * 4096 + (d >> 5) * 1024 + ((tok & 15) + 16 * ((d >> 2) & 3)) * 16 + (((d >> 4) & 1) * 4 + (d & 3)) * 2))); }
DI float v_el(const char* img, int key, int d) { return bf2f(ld16_dev((const u16*)(img + (key >> 5) * 8192 + (d >> 4) * 1024 + ((d & 15) + 16 * ((key >> 2) & 3)) * 16 + (((key >> 4) & 1) * 4 + (key & 3)) * 2))); }
DI void attn_slow(float* ql, const u16* bx, const u16* wq, char* ws, int h, int qi) {
    const int tid = (int)otid(), row = tid >> 1, dh = tid & 1;
    for (int i = tid; i < 128 * 128; i += 256) { const int rr = i >> 7, d = i & 127; float s = 0.f;
        for (int k = 0; k < 640; ++k) s += bf2f(bx[(size_t)(128 * qi + rr) * 640 + k]) * bf2f(wq[(size_t)k * 4096 + h * 128 + d]);
        ql[rr * 129 + d] = bf2f(f2bf(s * QSCALE)); }
    __syncthreads();
    const char* Ki = ws + WS_K + (size_t)h * HSTR; const char* Vi = ws + WS_V + (size_t)h * HSTR;
    float* acc = ql + 128 * 129 + tid * 65;
    for (int d = 0; d < 64; ++d) acc[d] = 0.f;
    float m = -3.0e38f, l = 0.f;
    for (int key = 0; key <= 128 * qi + row; ++key) {
        float s = 0.f;
        for (int d = 0; d < 128; ++d) s += ql[row * 129 + d] * kq_el(Ki, key, d);
        const float mn = fmaxf(m, s), al = exp2f(m - mn), p = bf2f(f2bf(exp2f(s - mn)));
        l = l * al + p; m = mn;
        for (int d = 0; d < 64; ++d) acc[d] = acc[d] * al + p * v_el(Vi, key, 64 * dh + d);
    }
    u16* co = (u16*)(ws + WS_CTX + (size_t)qi * CT_S + (2 * h + dh) * 16384 + (row >> 4) * 2048);
    for (int d = 0; d < 64; ++d) __hip_atomic_store(co + ((d >> 5) * 1024 + ((row & 15) + 16 * ((d >> 2) & 3)) * 16 + (((d >> 4) & 1) * 4 + (d & 3)) * 2) / 2, f2bf(acc[d] / l), __ATOMIC_RELAXED, __HIP_MEMORY_SCOPE_AGENT);
    asm volatile("s_waitcnt vmcnt(0)" ::: "memory");
    __syncthreads();
}

constexpr unsigned K3_STG = 20480;
struct OutC {
    ldsp py[2];
    unsigned vx, vy[5];
    unsigned m0y, sx, sy, ox, oy;
    bool im;
    unsigned nt, ne, ke, kl;
    __amdgpu_buffer_rsrc_t srdX, srdY, srdY2;
    s16x8 Y[2][10];
};
template <bool CP, int ST, int K32, int J> DI void o_read(OutC& c) {
    if constexpr (CP) c.Y[K32][J] = lds_b128(c.py[K32] + ST * K3_STG + J * 2048); else c.Y[K32][J] = lds_b128(c.py[0] + ST * K3_STG + K32 * 10240 + J * 1024);
}
template <bool CP> DI void o_src(OutC& c, unsigned t) {
    const unsigned oob = t < c.nt ? 0u : 0x40000000u;
    if constexpr (CP) { c.ox = c.sx + t * 16384u + oob; c.oy = (t >= 10u ? t - 10u : t) * 128u + oob; c.im = t >= 10u; }
    else { const unsigned u = t < c.ne ? t : t - c.ne, kk = (t < c.ne ? c.ke : c.kl) + (u >> 1) * 4u + (u & 1u); c.ox = c.sx + kk * 16384u + oob; c.oy = c.sy + kk * 81920u + oob; }
}
template <bool CP, int ST, int P> DI void o_vm(OutC& c) {
    if constexpr (P < 4) ld_acc<16 * ST + 4 * P, 1024 * P>(c.vx, c.srdX, c.ox);
    else if constexpr (CP) dma16(c.m0y + ST * K3_STG + (P - 4) * 1024, c.vy[P - 4], c.im ? c.srdY2 : c.srdY, c.oy);
    else if constexpr (P < 8) dma4<P - 4>(c.m0y + ST * K3_STG, c.vx, c.srdY, c.oy);
    else dma4<0>(c.m0y + ST * K3_STG + 4096u, c.vx, c.srdY, c.oy + 4096u);
}
template <bool CP, int ST, int G> DI void o_gap(OutC& c, unsigned t) {
    constexpr int S = G / 20, I = G % 20, n = I >> 1, m = I & 1, ax = 16 * ST + 8 * m + 4 * S;
    asm volatile("v_mfma_f32_16x16x32_bf16 a[%c1:%c2], %0, a[%c3:%c4], a[%c1:%c2]" ::"v"(c.Y[S][n]), "i"(64 + 4 * I), "i"(67 + 4 * I), "i"(ax), "i"(ax + 3));
    GAP();
    if constexpr (G == 20) { asm volatile("s_waitcnt vmcnt(14)" ::: "memory"); __builtin_amdgcn_s_waitcnt(0xC07F); __builtin_amdgcn_s_barrier(); asm volatile("" ::: "memory"); }
    if constexpr (I & 1) { if constexpr (S == 0) o_read<CP, ST, 1, n>(c); else o_read<CP, (ST + 1) & 3, 0, n>(c); }
    if constexpr (G == 0) o_src<CP>(c, t + 3);
    if constexpr (G == 24) ACC_GUARD();
    if constexpr (G >= 2 && (G & 3) == 2 && G < 38) o_vm<CP, (ST + 3) & 3, (G >> 2)>(c);
    GAP();
}
template <bool CP, int ST, int... Gs> DI void o_step_(OutC& c, unsigned t, iseq<Gs...>) { (o_gap<CP, ST, Gs>(c, t), ...); }
template <bool CP, int ST> DI void o_step(OutC& c, unsigned t) { o_step_<CP, ST>(c, t, make_iseq<40>{}); }
template <bool CP, int ST, int... Ps> DI void o_vm_all(OutC& c, iseq<Ps...>) { o_src<CP>(c, ST); (o_vm<CP, ST, Ps>(c), ...); }
template <bool CP, int... Js> DI void o_read0(OutC& c, iseq<Js...>) { (o_read<CP, 0, 0, Js>(c), ...); }
template <bool CP> DI void o_prologue(OutC& c, int nt) {
    c.nt = (unsigned)nt;
    asm volatile("s_nop 4" ::: "memory");
    o_vm_all<CP, 0>(c, make_iseq<9>{}); o_vm_all<CP, 1>(c, make_iseq<9>{}); o_vm_all<CP, 2>(c, make_iseq<9>{});
}
template <bool CP> DI void o_main(OutC& c, unsigned* fl_ptr, unsigned fl_need, unsigned ep) {
    a_zero_n(iseq<4, 5, 6, 7, 8>{});
    asm volatile("s_waitcnt vmcnt(%c0)\n\ts_barrier" ::"i"(CP ? 30 + 18 : 18) : "memory");
        o_read0<CP>(c, make_iseq<10>{});
    GAP();
    unsigned fl = 0;
    for (unsigned t = 0;; t += 4) {
        if constexpr (!CP) { if (t == 0) fl = flag_issue(fl_ptr); }
        o_step<CP, 0>(c, t); o_step<CP, 1>(c, t + 1);
        if (t + 2 >= c.nt) break;
        if constexpr (!CP) { if (t == 4) flag_check(fl, fl_ptr, fl_need, ep); }
        o_step<CP, 2>(c, t + 2); o_step<CP, 3>(c, t + 3);
        if (t + 4 >= c.nt) break;
    }
    asm volatile("s_nop 7\n\ts_waitcnt vmcnt(0)" ::: "memory");
    GAP();
    }
template <int M, int N> DI void o_slab1(unsigned voff, __amdgpu_buffer_rsrc_t srd, unsigned soff) {
    asm volatile("buffer_store_dwordx4 a[%c3:%c4], %0, %1, %2 offen offset:%c5 sc1" ::"v"(voff), "s"(srd), "s"(soff + (N >> 2) * 4096u), "i"(64 + 8 * N + 4 * M), "i"(67 + 8 * N + 4 * M), "i"((N & 3) * 1024) : "memory");
}
template <int M, int... Ns> DI void o_slab(unsigned voff, __amdgpu_buffer_rsrc_t srd, unsigned soff, iseq<Ns...>) { (o_slab1<M, Ns>(voff, srd, soff), ...); }
template <int M, int... Ns> DI void o_accs(f32x4 (&v)[10], iseq<Ns...>) { ((v[Ns] = a_read4<64 + 8 * Ns + 4 * M>()), ...); }
DI float rbf(float x) { return bf2f(f2bf(x)); }
DI void out_phase(char* lds, const u16* cre, const u16* cim, const u16* ax, const u16* dskip, char* ws, float* out, int x, int mt, int nt, unsigned ep) {
    const unsigned tid = otid(), lane = tid & 63u, wid = rfl(tid >> 6), r16 = lane & 15u, g = lane >> 4;
    const unsigned ldsb = (unsigned)(size_t)lds;
    OutC c;
        c.m0y = rfl(ldsb + wid * 5120u);
    c.vx = lane * 16u; c.sx = rfl(wid * 4096u);
    const int row0 = 128 * mt + 32 * (int)wid;
    if (x < 6) {
        const int s = x % 3, h0 = 11 * s, h1 = s == 2 ? 32 : h0 + 11, pe = mt >= 8 ? 0 : 1, he = h0 + ((h0 ^ pe) & 1), hl = h0 + ((h0 ^ pe ^ 1) & 1), nhe = (h1 - he + 1) >> 1, nhl = (h1 - hl + 1) >> 1;
        c.ne = 2u * nhe; c.ke = 2u * he; c.kl = 2u * hl;
        c.srdX = mk_srd(ws + WS_CTX + (size_t)mt * CT_S, 1048576u);
        c.srdY = mk_srd(ws + WS_WOT + nt * 20480, 64u * 81920u - (unsigned)nt * 20480u); c.srdY2 = c.srdY; c.sy = rfl(wid * 5120u); c.im = false;
        o_prologue<false>(c, 2 * (h1 - h0));
        c.py[0] = c.py[1] = (ldsp)(size_t)(ldsb + lane * 16u);
        o_main<false>(c, cnt_ptr(ws, C_CTX + (mt * 3 + s) * 2 + (pe ^ 1)), 4u * nhl, ep);
        const __amdgpu_buffer_rsrc_t srdS = mk_srd(ws + WS_SLAB + (size_t)(mt * 4 + nt) * 245760, 245760u);
        unsigned* cnt = cnt_ptr(ws, C_RED + mt * 4 + nt);
        asm volatile("s_nop 4" ::: "memory");
        if ((2 * (int)wid) % 3 != s) o_slab<0>(c.vx, srdS, rfl((unsigned)(s * 8 + 2 * (int)wid) * 10240u), make_iseq<10>{});
        if ((2 * (int)wid + 1) % 3 != s) o_slab<1>(c.vx, srdS, rfl((unsigned)(s * 8 + 2 * (int)wid + 1) * 10240u), make_iseq<10>{});
        asm volatile("s_waitcnt vmcnt(0)" ::: "memory");
        if (lane == 0) sig_tag(cnt + s * 4 + (int)wid, ep);
        const int s1 = s == 0 ? 1 : 0, s2 = s == 2 ? 1 : 2, mo = (2 * (int)wid) % 3 == s ? 0 : ((2 * (int)wid + 1) % 3 == s ? 1 : -1);
        if (mo >= 0) {
            poll_tags(cnt, 12u, ep);
            if (blockIdx.x == 227 && tid == 0) sig_tag(cnt_ptr(ws, 0), ep);
            asm volatile("" ::: "memory");
            const unsigned rb = 2u * wid + (unsigned)mo;
            u32x4 p1[10], p2[10];
#pragma unroll
            for (int n = 0; n < 10; ++n) { p1[n] = __builtin_amdgcn_raw_buffer_load_b128(srdS, c.vx + n * 1024u, ((unsigned)s1 * 8u + rb) * 10240u, 16); p2[n] = __builtin_amdgcn_raw_buffer_load_b128(srdS, c.vx + n * 1024u, ((unsigned)s2 * 8u + rb) * 10240u, 16); }
            f32x4 av[10];
            if (mo) o_accs<1>(av, make_iseq<10>{}); else o_accs<0>(av, make_iseq<10>{});
            float* orow = out + (size_t)(row0 + 16 * mo + r16) * 1280 + 640 + nt * 160 + 4 * g;
#pragma unroll
            for (int n = 0; n < 10; ++n) {
                const f32x4 a = av[n], b1 = __builtin_bit_cast(f32x4, p1[n]), b2 = __builtin_bit_cast(f32x4, p2[n]);
                f32x4 v = s == 0 ? (a + b1) + b2 : (s == 1 ? (b1 + a) + b2 : (b1 + b2) + a);
                v[0] = rbf(v[0]); v[1] = rbf(v[1]); v[2] = rbf(v[2]); v[3] = rbf(v[3]);
                *(f32x4*)(orow + 16 * n) = v;
            }
        }
    } else {
        c.srdX = mk_srd(ws + WS_HT + (size_t)mt * HT_S, 327680u);
#pragma unroll
        for (int p = 0; p < 5; ++p) { const unsigned row = 40u * wid + 8u * p + (lane >> 3); c.vy[p] = row * 1280u + (((lane & 7u) ^ ((row >> 1) & 7u)) * 16u); }
        c.srdY = mk_srd((const char*)cre + nt * 160 * 1280, 160u * 1280u); c.srdY2 = mk_srd((const char*)cim + nt * 160 * 1280, 160u * 1280u);
        o_prologue<true>(c, 20);
#pragma unroll
        for (int k = 0; k < 2; ++k) c.py[k] = (ldsp)(size_t)(ldsb + r16 * 128u + (((4u * k + g) ^ ((r16 >> 1) & 7u)) * 16u));
        u32x2 xv[2][10], dk[10];
        { const __amdgpu_buffer_rsrc_t srdA = mk_srd(ax + (size_t)row0 * 640 + nt * 160, 32u * 1280u - (unsigned)(nt * 320)), srdD = mk_srd(dskip + nt * 160, 320u);
          const unsigned va = r16 * 1280u + g * 8u, vd = g * 8u;
          asm volatile("s_nop 4" ::: "memory");
#pragma unroll
          for (int n = 0; n < 10; ++n) { asm volatile("buffer_load_dwordx2 %0, %1, %2, 0 offen offset:%c3" : "=v"(dk[n]) : "v"(vd), "s"(srdD), "i"(32 * n) : "memory");
#pragma unroll
              for (int m = 0; m < 2; ++m) asm volatile("buffer_load_dwordx2 %0, %1, %2, %3 offen offset:%c4" : "=v"(xv[m][n]) : "v"(va), "s"(srdA), "s"(m * 20480), "i"(32 * n) : "memory"); } }
        o_main<true>(c, nullptr, 0u, ep);
#pragma unroll
        for (int m = 0; m < 2; ++m) {
            f32x4 av[10];
            if (m) o_accs<1>(av, make_iseq<10>{}); else o_accs<0>(av, make_iseq<10>{});
            float* orow = out + (size_t)(row0 + 16 * m + r16) * 1280 + nt * 160 + 4 * g;
#pragma unroll
            for (int n = 0; n < 10; ++n) {
                asm volatile("" : "+v"(xv[m][n]), "+v"(dk[n]));
                const u32x2 a = xv[m][n], d2 = dk[n];
                f32x4 v = av[n];
                v[0] += rbf(__uint_as_float(a[0] << 16) * __uint_as_float(d2[0] << 16)); v[1] += rbf(__uint_as_float(a[0] & 0xffff0000u) * __uint_as_float(d2[0] & 0xffff0000u));
                v[2] += rbf(__uint_as_float(a[1] << 16) * __uint_as_float(d2[1] << 16)); v[3] += rbf(__uint_as_float(a[1] & 0xffff0000u) * __uint_as_float(d2[1] & 0xffff0000u));
                *(f32x4*)(orow + 16 * n) = v;
            }
        }
    }
        }

#define QL_TAB 0x65310742u
#define QH_TAB 0x89BCEADFu
struct Role { int x, idx, h, j, qA, qB, mt, nt, ut; bool isU; };
DI Role role() {
    int b = blockIdx.x; asm volatile("" : "+s"(b));
    Role r; r.x = b & 7; r.idx = b >> 3; r.h = r.x * 4 + (r.idx >> 3); r.j = r.idx & 7;
    const int ql = (int)((QL_TAB >> (4 * r.j)) & 15u), qh = (int)((QH_TAB >> (4 * r.j)) & 15u);
    r.qA = (r.h & 1) ? ql : qh; r.qB = (r.h & 1) ? qh : ql;
    r.mt = ((0xB8 >> r.x) & 1) * 8 + (r.idx >> 2); r.nt = r.idx & 3;
    r.isU = r.j >= 3; r.ut = r.isU ? r.h * 5 + r.j - 3 : r.h * 3 + r.j;
    return r;
}
DI unsigned* out_flag(unsigned& need) {
    char* ws = kargs()->ws; const Role r = role();
    const int s3 = r.x % 3, pe = r.mt >= 8 ? 0 : 1, h0 = 11 * s3, h1 = s3 == 2 ? 32 : h0 + 11, he = h0 + ((h0 ^ pe) & 1);
    need = r.x < 6 ? 4u * (unsigned)((h1 - he + 1) >> 1) : 40u;
    return cnt_ptr(ws, r.x < 6 ? C_CTX + (r.mt * 3 + s3) * 2 + pe : C_H + r.mt);
}
__global__ __launch_bounds__(256, 1) void mega(KArgs args_) {
    extern __shared__ __attribute__((aligned(16))) char lds[];
         unsigned genv; asm volatile("global_load_dword %0, %1, off sc1" : "=v"(genv) : "v"(cnt_ptr(kargs()->ws, 0)) : "memory");
    UKeep uk;
    KvOut ko; QpC qc; UC uc; unsigned rdy;
    { const kargp a = kargs(); const Role r = role();
      auto hook = [&]() { const kargp a2 = kargs(); const Role r2 = role();
          if (r2.isU) u_pre(uc, lds, a2->ax, a2->bre, a2->bim, a2->lre, a2->lim, a2->logdt, r2.ut); else wo_pre(lds, a2->wo, r2.ut); };
      kv_tile(hook, ko, lds, a->ckv, a->wuk, a->wuv, a->ws, r.h, r.j); }
    asm volatile("" : "+v"(genv));
    const unsigned ep = rfl(genv) * 0x9E3779B1u + 0x7F4A7C15u;
    { const kargp a = kargs(); const Role r = role();
      auto next = [&]() { const kargp a2 = kargs(); const Role r2 = role(); q_pro(qc, lds, a2->bx, a2->wq, r2.h, r2.qA, r2.qB); };
      if (r.isU) u_tile(uc, uk, ko, lds, a->ws, r.ut, cnt_ptr(a->ws, C_KV + r.h) + 4 * r.j, ep, next);
      else wo_tile(uk, ko, lds, next);
      q_pro2(qc); }
    { const kargp a = kargs(); const Role r = role();
      qc.ko = ko;
      rdy = q_main(qc, lds, a->ws, r.h, uk, r.isU ? r.ut : -1, r.isU ? nullptr : cnt_ptr(a->ws, C_KV + r.h) + 4 * r.j, r.isU ? cnt_ptr(a->ws, C_U + r.ut % 10) + r.ut / 10 : nullptr, ep);
      if (r.isU) u_fin(uk, lds, a->ws, r.ut); else wo_fin(uk, a->ws, r.ut); }
    unsigned badm;
    { const kargp a = kargs(); const Role r = role();
      badm = attn_phase(lds, a->ws, r.h, r.qA, r.qB, rdy, r.isU ? cnt_ptr(a->ws, C_H + r.ut / 10) + 4 * (r.ut % 10) : (r.ut < 64 ? cnt_ptr(a->ws, C_WO) + r.ut : nullptr), r.isU ? 1u : 0u, ep); }
    {
        __builtin_amdgcn_s_waitcnt(0xC07F); __builtin_amdgcn_s_barrier(); asm volatile("" ::: "memory");
        volatile LAS unsigned* orw = (volatile LAS unsigned*)(size_t)((unsigned)(size_t)lds + 163824u);
        if ((threadIdx.x & 63) == 0) orw[threadIdx.x >> 6] = badm;
        __builtin_amdgcn_s_waitcnt(0xC07F); __builtin_amdgcn_s_barrier(); asm volatile("" ::: "memory");
        const unsigned any = rfl(orw[0] | orw[1] | orw[2] | orw[3]);
        if (__builtin_expect(any != 0, 0)) {
            const kargp a = kargs(); const Role r = role(); char* ws = a->ws;
            for (int p = 0; p < 2; ++p) if ((any >> p) & 1u) attn_slow((float*)lds, a->bx, a->wq, ws, r.h, p ? r.qB : r.qA);
            if ((threadIdx.x & 63) == 0) { const int so = C_CTX + (r.h / 11) * 2 + (r.h & 1), sw = ((r.h % 11) >> 1) * 4 + (int)(threadIdx.x >> 6);
                if (badm & 1u) sig_tag(cnt_ptr(ws, so + r.qA * 6) + sw, ep); if (badm & 2u) sig_tag(cnt_ptr(ws, so + r.qB * 6) + sw, ep); }
            if (badm & 4u) { unsigned need; unsigned* fo = out_flag(need); poll_tags(fo, need, ep); poll_tags(cnt_ptr(ws, C_WO), 64u, ep); }
            __syncthreads();
        }
    }
    { const kargp a = kargs(); const Role r = role();
      out_phase(lds, a->cre, a->cim, a->ax, a->dskip, a->ws, a->out, r.x, r.mt, r.nt, ep); }
    asm volatile(".Lmega_end:");
}
extern "C" void kernel_launch(void* const* d_in, const int* in_sizes, int n_in, void* d_out, int out_size, void* d_ws, size_t ws_size, hipStream_t stream) {
    (void)in_sizes; (void)n_in; (void)out_size; (void)ws_size;
    (void)hipFuncSetAttribute((const void*)mega, hipFuncAttributeMaxDynamicSharedMemorySize, 163840);
    KArgs a;
    a.ax = (const u16*)d_in[0]; a.lre = (const u16*)d_in[1]; a.lim = (const u16*)d_in[2]; a.bre = (const u16*)d_in[3]; a.bim = (const u16*)d_in[4]; a.cre = (const u16*)d_in[5]; a.cim = (const u16*)d_in[6];
    a.logdt = (const u16*)d_in[7]; a.dskip = (const u16*)d_in[8]; a.bx = (const u16*)d_in[9]; a.ckv = (const u16*)d_in[10]; a.wq = (const u16*)d_in[11]; a.wuk = (const u16*)d_in[12]; a.wuv = (const u16*)d_in[13];
    a.wo = (const u16*)d_in[14]; a.out = (float*)d_out; a.ws = (char*)d_ws;
    mega<<<256, 256, 163840, stream>>>(a);
}
```
